```python
import torch

def _softmax(x, axis=-1):
    return torch.softmax(x, dim=axis)
def _gelu(x):
    return torch.nn.functional.gelu(x, approximate="tanh")
def _silu(x):
    return torch.nn.functional.silu(x)
def _sigmoid(x):
    return torch.sigmoid(x)
def _layernorm(x, axis=-1, eps=1e-5):
    m = x.mean(dim=axis, keepdim=True)
    v = x.var(dim=axis, keepdim=True, unbiased=False)
    return (x - m) / torch.sqrt(v + eps)
def _scatter_add(tgt, idx, src, axis=0):
    return tgt.clone().index_add_(axis, idx, src)
def _segment_sum(src, seg, n):
    out = torch.zeros((n,) + src.shape[1:], dtype=src.dtype)
    return out.index_add_(0, seg, src)
def _pad(x, pw):
    flat = tuple(v for pair in reversed(pw) for v in pair)
    return torch.nn.functional.pad(x, flat)

kernel_name = 'standalone__weight_only_int_omniquant__prompt'

def setup_inputs():

    _w = (lambda a: (lambda t: t.to(torch.bfloat16) if t.is_floating_point() else t)(torch.as_tensor(a)))
    B, S, D, F = 1, 2048, 576, 4608
    bits = 8
    G = -1 if -1 > 0 else D
    D = (D // G) * G
    ng = D // G
    qmax = (1 << bits) - 1
    qw = torch.randint(0, qmax + 1, (D, F)).to(torch.int32)
    scales = (torch.abs((lambda *s: torch.randn(*s, dtype=torch.bfloat16) * 0.05)(ng, F)) * 0.05 + 0.01).to(float)
    zeros = torch.randint(0, qmax + 1, (ng, F)).to(float)
    act_scale = (torch.abs((lambda *s: torch.randn(*s, dtype=torch.bfloat16) * 0.05)(D)) * 0.5 + 0.5).to(float)
    return {
        "x": _w((lambda *s: torch.randn(*s, dtype=torch.bfloat16) * 0.05)(B, S, D)),
        "qw": _w(qw),
        "scales": _w(scales),
        "zeros": _w(zeros),
        "act_scale": _w(act_scale),
    }

def reference(x, qw, scales, zeros, act_scale):
    B, S, D = x.shape
    ng, F = scales.shape
    G = D // ng
    s = (lambda x, n, axis: torch.repeat_interleave(x, n, dim=axis))(scales, G, axis=0)
    z = (lambda x, n, axis: torch.repeat_interleave(x, n, dim=axis))(zeros, G, axis=0)
    w = ((lambda x, dt: x.to(dt))(qw, x.dtype) - z) * s
    xs = x * (lambda x, dt: x.to(dt))(act_scale, x.dtype)
    return torch.einsum("bsd,df->bsf", xs, w)
```

```cpp
#include <hip/hip_runtime.h>
#include <hip/hip_bf16.h>
#include <stdint.h>

typedef short bf16x8 __attribute__((ext_vector_type(8)));
typedef float f32x4 __attribute__((ext_vector_type(4)));
typedef unsigned short u16;

constexpr int M_ = 2048, K_ = 576, N_ = 4608;

__device__ __forceinline__ float bf2f(u16 v) { return __uint_as_float(((unsigned)v) << 16); }
__device__ __forceinline__ u16 f2bf(float f) { __hip_bfloat16 b = __float2bfloat16(f); return *reinterpret_cast<u16*>(&b); }

__global__ __launch_bounds__(256) void v0_kernel(const u16* __restrict__ x, const int* __restrict__ qw,
                                                 const u16* __restrict__ sc, const u16* __restrict__ zr,
                                                 const u16* __restrict__ as, u16* __restrict__ out) {
    __shared__ __attribute__((aligned(16))) u16 As[64][40];
    __shared__ __attribute__((aligned(16))) u16 Bs[64][40];
    const int tid = threadIdx.x, lane = tid & 63, wid = tid >> 6;
    const int bm = blockIdx.y * 64, bn = blockIdx.x * 64;
    const int wr = wid >> 1, wc = wid & 1;
    f32x4 acc[2][2] = {};
    for (int k0 = 0; k0 < K_; k0 += 32) {
        for (int i = tid; i < 64 * 32; i += 256) {
            int r = i >> 5, c = i & 31;
            As[r][c] = f2bf(bf2f(x[(size_t)(bm + r) * K_ + k0 + c]) * bf2f(as[k0 + c]));
        }
        for (int i = tid; i < 64 * 32; i += 256) {
            int k = i >> 6, n = i & 63;
            float q = bf2f(f2bf((float)qw[(size_t)(k0 + k) * N_ + bn + n]));
            float d = bf2f(f2bf(q - bf2f(zr[bn + n])));
            Bs[n][k] = f2bf(d * bf2f(sc[bn + n]));
        }
        __syncthreads();
        for (int m = 0; m < 2; ++m)
            for (int n = 0; n < 2; ++n) {
                bf16x8 a = *reinterpret_cast<const bf16x8*>(&As[wr * 32 + m * 16 + (lane & 15)][(lane >> 4) * 8]);
                bf16x8 b = *reinterpret_cast<const bf16x8*>(&Bs[wc * 32 + n * 16 + (lane & 15)][(lane >> 4) * 8]);
                acc[m][n] = __builtin_amdgcn_mfma_f32_16x16x32_bf16(a, b, acc[m][n], 0, 0, 0);
            }
        __syncthreads();
    }
    for (int m = 0; m < 2; ++m)
        for (int n = 0; n < 2; ++n)
            for (int j = 0; j < 4; ++j)
                out[(size_t)(bm + wr * 32 + m * 16 + (lane >> 4) * 4 + j) * N_ + bn + wc * 32 + n * 16 + (lane & 15)] = f2bf(acc[m][n][j]);
}

extern "C" void kernel_launch(void* const* d_in, const int* in_sizes, int n_in,
                              void* d_out, int out_size, void* d_ws, size_t ws_size,
                              hipStream_t stream) {
    v0_kernel<<<dim3(N_ / 64, M_ / 64), 256, 0, stream>>>((const u16*)d_in[0], (const int*)d_in[1], (const u16*)d_in[2],
                                                          (const u16*)d_in[3], (const u16*)d_in[4], (u16*)d_out);
}
```

```cpp
#include <hip/hip_runtime.h>
#include <hip/hip_bf16.h>
#include <stdint.h>

typedef short bf16x8 __attribute__((ext_vector_type(8)));
typedef float f32x4 __attribute__((ext_vector_type(4)));
typedef unsigned short u16;

constexpr int M_ = 2048, K_ = 576, N_ = 4608;

__device__ __forceinline__ float bf2f(u16 v) { return __uint_as_float(((unsigned)v) << 16); }
__device__ __forceinline__ u16 f2bf(float f) { __hip_bfloat16 b = __float2bfloat16(f); return *reinterpret_cast<u16*>(&b); }

__global__ __launch_bounds__(256) void v0_kernel(const u16* __restrict__ x, const int* __restrict__ qw,
                                                 const u16* __restrict__ sc, const u16* __restrict__ zr,
                                                 const u16* __restrict__ as, u16* __restrict__ out) {
    __shared__ __attribute__((aligned(16))) u16 As[64][40];
    __shared__ __attribute__((aligned(16))) u16 Bs[64][40];
    const int tid = threadIdx.x, lane = tid & 63, wid = tid >> 6;
    const int bm = blockIdx.y * 64, bn = blockIdx.x * 64;
    const int wr = wid >> 1, wc = wid & 1;
    f32x4 acc[2][2] = {};
    for (int k0 = 0; k0 < K_; k0 += 32) {
        for (int i = tid; i < 64 * 32; i += 256) {
            int r = i >> 5, c = i & 31;
            As[r][c] = f2bf(bf2f(x[(size_t)(bm + r) * K_ + k0 + c]) * bf2f(as[k0 + c]));
        }
        for (int i = tid; i < 64 * 32; i += 256) {
            int k = i >> 6, n = i & 63;
            float q = bf2f(f2bf((float)qw[(size_t)(k0 + k) * N_ + bn + n]));
            float d = bf2f(f2bf(q - bf2f(zr[bn + n])));
            Bs[n][k] = f2bf(d * bf2f(sc[bn + n]));
        }
        __syncthreads();
        for (int m = 0; m < 2; ++m)
            for (int n = 0; n < 2; ++n) {
                bf16x8 a = *reinterpret_cast<const bf16x8*>(&As[wr * 32 + m * 16 + (lane & 15)][(lane >> 4) * 8]);
                bf16x8 b = *reinterpret_cast<const bf16x8*>(&Bs[wc * 32 + n * 16 + (lane & 15)][(lane >> 4) * 8]);
                acc[m][n] = __builtin_amdgcn_mfma_f32_16x16x32_bf16(a, b, acc[m][n], 0, 0, 0);
            }
        __syncthreads();
    }
    for (int m = 0; m < 2; ++m)
        for (int n = 0; n < 2; ++n)
            for (int j = 0; j < 4; ++j)
                out[(size_t)(bm + wr * 32 + m * 16 + (lane >> 4) * 4 + j) * N_ + bn + wc * 32 + n * 16 + (lane & 15)] = f2bf(acc[m][n][j]);
}


typedef unsigned u32x4 __attribute__((ext_vector_type(4)));
__global__ __launch_bounds__(512) void k_read(const u32x4* x, const u32x4* qw, unsigned* ws, unsigned* out, int touch) {
    const int t = blockIdx.x * 512 + threadIdx.x;
    u32x4 acc = {0u, 0u, 0u, 0u};
    if (t < 147456) acc ^= x[t];
#pragma unroll
    for (int i = 0; i < 5; ++i) acc ^= qw[t + i * 131072];
    if (t < 663552 - 5 * 131072) acc ^= qw[t + 5 * 131072];
    unsigned s = acc.x ^ acc.y ^ acc.z ^ acc.w;
    ws[t] = s;
    if (touch && t < 4608) out[t * 1024] = 0;
}

extern "C" void kernel_launch(void* const* d_in, const int* in_sizes, int n_in,
                              void* d_out, int out_size, void* d_ws, size_t ws_size,
                              hipStream_t stream) {
    k_read<<<256, 512, 0, stream>>>((const u32x4*)d_in[0], (const u32x4*)d_in[1], (unsigned*)d_ws, (unsigned*)d_out, 1);
    v0_kernel<<<dim3(N_ / 64, M_ / 64), 256, 0, stream>>>((const u16*)d_in[0], (const int*)d_in[1], (const u16*)d_in[2],
                                                          (const u16*)d_in[3], (const u16*)d_in[4], (u16*)d_out);
}
```

```cpp
#include <hip/hip_runtime.h>
#include <stdint.h>

__global__ __launch_bounds__(512, 2) void oq_kernel(const void* __restrict__ x, const void* __restrict__ qw,
        const void* __restrict__ sc, const void* __restrict__ zr, const void* __restrict__ as, void* __restrict__ out) {
    __shared__ __attribute__((aligned(1024))) char lds[158976];
    const unsigned long long ka = (unsigned long long)__builtin_amdgcn_kernarg_segment_ptr();
    const unsigned blk = blockIdx.x;
    const unsigned wid = __builtin_amdgcn_readfirstlane(threadIdx.x >> 6);
    const unsigned ldsb = (unsigned)(size_t)(__attribute__((address_space(3))) char*)lds;
    asm volatile(
        "s_load_dwordx8 s[12:19], %0, 0\n"
        "s_load_dwordx4 s[20:23], %0, 0x20\n"
        "s_lshr_b32 s33, %1, 3\n"
        "s_and_b32 s34, %1, 1\n"
        "s_bfe_u32 s35, %1, 0x20001\n"
        "s_mul_i32 s36, s33, 37\n"
        "s_lshr_b32 s36, s36, 8\n"
        "s_mul_i32 s37, s36, 7\n"
        "s_sub_u32 s37, s33, s37\n"
        "s_mul_i32 s38, s34, 5\n"
        "s_add_u32 s38, s38, s36\n"
        "s_lshl_b32 s39, s34, 2\n"
        "s_cmp_lt_u32 s33, 28\n"
        "s_cselect_b32 s38, s38, 4\n"
        "s_cselect_b32 s39, 0, s39\n"
        "s_add_u32 s37, s37, s39\n"
        "s_cmp_ge_u32 s37, 7\n"
        "s_cbranch_scc1 L_end_%=\n"
        "s_mul_i32 s26, s35, 9\n"
        "s_add_u32 s26, s26, s38\n"
        "s_min_u32 s39, s37, 2\n"
        "s_mul_i32 s28, s37, 18\n"
        "s_add_u32 s28, s28, s39\n"
        "s_cmp_lt_u32 s37, 2\n"
        "s_cselect_b32 s29, 19, 18\n"
        "s_min_u32 s27, s28, 109\n"
        "s_add_u32 s39, s37, s38\n"
        "s_mul_i32 s36, s39, 86\n"
        "s_lshr_b32 s36, s36, 8\n"
        "s_mul_i32 s36, s36, 3\n"
        "s_sub_u32 s39, s39, s36\n"
        "s_mov_b32 s30, 0x76543210\n"
        "s_mov_b32 s31, 8\n"
        "s_cmp_eq_u32 s39, 1\n"
        "s_cselect_b32 s30, 0x87354021, s30\n"
        "s_cselect_b32 s31, 6, s31\n"
        "s_cmp_eq_u32 s39, 2\n"
        "s_cselect_b32 s30, 0x68435102, s30\n"
        "s_cselect_b32 s31, 7, s31\n"
        "s_mul_i32 s39, s38, 2\n"
        "s_sub_u32 s36, s39, 11\n"
        "s_min_u32 s39, s39, s36\n"
        "s_and_b32 s82, %2, 3\n"
        "s_mov_b32 s83, %3\n"
        "v_mbcnt_lo_u32_b32 v2, -1, 0\n"
        "v_mbcnt_hi_u32_b32 v2, -1, v2\n"
        "s_getpc_b64 s[98:99]\n"
        "L_pc_%=:\n"
        "s_lshl_b32 s78, s33, 10\n"
        "s_add_u32 s98, s98, s78\n"
        "s_addc_u32 s99, s99, 0\n"
        "s_cmp_lg_u32 %2, 7\n"
        "s_cbranch_scc1 L_nopf_%=\n"
        "s_cmp_ge_u32 s33, (L_end_%=-L_pc_%=)/1024\n"
        "s_cbranch_scc1 L_nopf_%=\n"
        "v_lshlrev_b32 v3, 4, v2\n"
        "global_load_dwordx4 v[92:95], v3, s[98:99]\n"
        "L_nopf_%=:\n"
        "s_cmp_lt_u32 %2, 4\n"
        "s_cbranch_scc1 L_cons_%=\n"
        "s_setprio 1\n"
        "s_lshl_b32 s78, s82, 1\n"
        "v_lshrrev_b32 v4, 5, v2\n"
        "v_add_u32 v4, s78, v4\n"
        "v_and_b32 v5, 31, v2\n"
        "s_mov_b32 s79, 147456\n"
        "v_mul_lo_u32 v6, v4, s79\n"
        "v_lshl_add_u32 v72, v5, 4, v6\n"
        "s_lshl_b32 s78, s26, 8\n"
        "v_lshl_add_u32 v6, v5, 3, s78\n"
        "v_lshlrev_b32 v7, 4, v4\n"
        "v_add_u32 v73, 18432, v72\n"
        "v_add_u32 v74, 18432, v73\n"
        "v_add_u32 v75, 18432, v74\n"
        "v_add_u32 v76, 18432, v75\n"
        "v_add_u32 v77, 18432, v76\n"
        "v_add_u32 v78, 18432, v77\n"
        "v_add_u32 v79, 18432, v78\n"
        "v_lshrrev_b32 v8, 2, v4\n"
        "v_lshlrev_b32 v8, 2, v8\n"
        "v_lshrrev_b32 v9, 3, v5\n"
        "v_add_u32 v8, v8, v9\n"
        "v_lshlrev_b32 v8, 1, v8\n"
        "v_and_b32 v9, 1, v5\n"
        "v_add_u32 v8, v8, v9\n"
        "v_mul_u32_u24 v8, 1088, v8\n"
        "v_and_b32 v9, 3, v4\n"
        "v_lshlrev_b32 v9, 4, v9\n"
        "v_bfe_u32 v10, v5, 1, 2\n"
        "v_add_u32 v9, v9, v10\n"
        "v_lshl_add_u32 v8, v9, 4, v8\n"
        "s_add_u32 s78, s83, 49152\n"
        "v_add_u32 v88, s78, v8\n"
        "s_add_u32 s78, s83, 101376\n"
        "v_lshl_add_u32 v89, v4, 5, s78\n"
        "s_bfe_u32 s81, s30, 262144\n"
        "s_mul_i32 s60, s81, 1179648\n"
        "s_lshl_b32 s81, s81, 7\n"
        "s_lshl_b32 s78, s26, 9\n"
        "s_waitcnt lgkmcnt(0)\n"
        "s_mov_b32 s56, s16\n"
        "s_and_b32 s57, s17, 0xffff\n"
        "s_mov_b32 s58, 9216\n"
        "s_mov_b32 s59, 0x20000\n"
        "buffer_load_dwordx2 v[40:41], v6, s[56:59], 0 offen\n"
        "s_mov_b32 s88, s18\n"
        "s_and_b32 s89, s19, 0xffff\n"
        "s_mov_b32 s90, 9216\n"
        "s_mov_b32 s91, 0x20000\n"
        "buffer_load_dwordx2 v[42:43], v6, s[88:91], 0 offen\n"
        "s_mov_b32 s40, s20\n"
        "s_and_b32 s41, s21, 0xffff\n"
        "s_mov_b32 s42, 1152\n"
        "s_mov_b32 s43, 0x20000\n"
        "buffer_load_dwordx4 v[44:47], v7, s[40:43], s81 offen\n"
        "s_add_u32 s44, s14, s78\n"
        "s_addc_u32 s45, s15, 0\n"
        "s_and_b32 s45, s45, 0xffff\n"
        "s_sub_u32 s46, 10616832, s78\n"
        "s_mov_b32 s47, 0x20000\n"
        "buffer_load_dwordx4 v[160:163], v72, s[44:47], s60 offen\n"
        "buffer_load_dwordx4 v[164:167], v73, s[44:47], s60 offen\n"
        "buffer_load_dwordx4 v[168:171], v74, s[44:47], s60 offen\n"
        "buffer_load_dwordx4 v[172:175], v75, s[44:47], s60 offen\n"
        "buffer_load_dwordx4 v[176:179], v76, s[44:47], s60 offen\n"
        "buffer_load_dwordx4 v[180:183], v77, s[44:47], s60 offen\n"
        "buffer_load_dwordx4 v[184:187], v78, s[44:47], s60 offen\n"
        "buffer_load_dwordx4 v[188:191], v79, s[44:47], s60 offen\n"
        "s_bfe_u32 s81, s30, 262144\n"
        "s_mul_i32 s60, s81, 1179648\n"
        "s_lshl_b32 s69, s81, 7\n"
        "s_bfe_u32 s81, s30, 262148\n"
        "s_mul_i32 s61, s81, 1179648\n"
        "s_lshl_b32 s70, s81, 7\n"
        "s_bfe_u32 s81, s30, 262152\n"
        "s_mul_i32 s62, s81, 1179648\n"
        "s_lshl_b32 s71, s81, 7\n"
        "s_bfe_u32 s81, s30, 262156\n"
        "s_mul_i32 s63, s81, 1179648\n"
        "s_lshl_b32 s72, s81, 7\n"
        "s_bfe_u32 s81, s30, 262160\n"
        "s_mul_i32 s64, s81, 1179648\n"
        "s_lshl_b32 s73, s81, 7\n"
        "s_bfe_u32 s81, s30, 262164\n"
        "s_mul_i32 s65, s81, 1179648\n"
        "s_lshl_b32 s74, s81, 7\n"
        "s_bfe_u32 s81, s30, 262168\n"
        "s_mul_i32 s66, s81, 1179648\n"
        "s_lshl_b32 s75, s81, 7\n"
        "s_bfe_u32 s81, s30, 262172\n"
        "s_mul_i32 s67, s81, 1179648\n"
        "s_lshl_b32 s76, s81, 7\n"
        "s_mov_b32 s81, s31\n"
        "s_mul_i32 s68, s81, 1179648\n"
        "s_lshl_b32 s77, s81, 7\n"
        "s_waitcnt vmcnt(8)\n"
        "v_lshlrev_b32 v16, 16, v40\n"
        "v_and_b32 v17, 0xffff0000, v40\n"
        "v_lshlrev_b32 v18, 16, v41\n"
        "v_and_b32 v19, 0xffff0000, v41\n"
        "v_lshlrev_b32 v20, 16, v42\n"
        "v_and_b32 v21, 0xffff0000, v42\n"
        "v_lshlrev_b32 v22, 16, v43\n"
        "v_and_b32 v23, 0xffff0000, v43\n"
        "v_lshlrev_b32 v24, 16, v44\n"
        "v_and_b32 v25, 0xffff0000, v44\n"
        "v_lshlrev_b32 v26, 16, v45\n"
        "v_and_b32 v27, 0xffff0000, v45\n"
        "v_lshlrev_b32 v28, 16, v46\n"
        "v_and_b32 v29, 0xffff0000, v46\n"
        "v_lshlrev_b32 v30, 16, v47\n"
        "v_and_b32 v31, 0xffff0000, v47\n"
        "v_mul_f32 v20, v20, v16\n"
        "v_mul_f32 v21, v21, v17\n"
        "v_mul_f32 v22, v22, v18\n"
        "v_mul_f32 v23, v23, v19\n"
        "s_waitcnt vmcnt(0)\n"
        "v_cvt_f32_i32 v40, v160\n"
        "v_cvt_f32_i32 v41, v164\n"
        "v_cvt_f32_i32 v42, v168\n"
        "v_cvt_f32_i32 v43, v172\n"
        "v_cvt_f32_i32 v44, v176\n"
        "v_cvt_f32_i32 v45, v180\n"
        "v_cvt_f32_i32 v46, v184\n"
        "buffer_load_dwordx4 v[192:195], v72, s[44:47], s62 offen\n"
        "v_cvt_f32_i32 v47, v188\n"
        "v_fma_f32 v40, v40, v16, -v20\n"
        "v_fma_f32 v41, v41, v16, -v20\n"
        "v_fma_f32 v42, v42, v16, -v20\n"
        "v_fma_f32 v43, v43, v16, -v20\n"
        "v_fma_f32 v44, v44, v16, -v20\n"
        "v_fma_f32 v45, v45, v16, -v20\n"
        "v_fma_f32 v46, v46, v16, -v20\n"
        "v_fma_f32 v47, v47, v16, -v20\n"
        "v_mul_f32 v40, v40, v24\n"
        "v_mul_f32 v41, v41, v25\n"
        "v_mul_f32 v42, v42, v26\n"
        "v_mul_f32 v43, v43, v27\n"
        "v_mul_f32 v44, v44, v28\n"
        "buffer_load_dwordx4 v[196:199], v73, s[44:47], s62 offen\n"
        "v_mul_f32 v45, v45, v29\n"
        "v_mul_f32 v46, v46, v30\n"
        "v_mul_f32 v47, v47, v31\n"
        "v_cvt_pk_bf16_f32 v56, v40, v41\n"
        "v_cvt_pk_bf16_f32 v57, v42, v43\n"
        "v_cvt_pk_bf16_f32 v58, v44, v45\n"
        "v_cvt_pk_bf16_f32 v59, v46, v47\n"
        "ds_write_b128 v88, v[56:59] offset:0\n"
        "v_cvt_f32_i32 v48, v161\n"
        "v_cvt_f32_i32 v49, v165\n"
        "v_cvt_f32_i32 v50, v169\n"
        "v_cvt_f32_i32 v51, v173\n"
        "v_cvt_f32_i32 v52, v177\n"
        "v_cvt_f32_i32 v53, v181\n"
        "v_cvt_f32_i32 v54, v185\n"
        "buffer_load_dwordx4 v[200:203], v74, s[44:47], s62 offen\n"
        "v_cvt_f32_i32 v55, v189\n"
        "v_fma_f32 v48, v48, v17, -v21\n"
        "v_fma_f32 v49, v49, v17, -v21\n"
        "v_fma_f32 v50, v50, v17, -v21\n"
        "v_fma_f32 v51, v51, v17, -v21\n"
        "v_fma_f32 v52, v52, v17, -v21\n"
        "v_fma_f32 v53, v53, v17, -v21\n"
        "v_fma_f32 v54, v54, v17, -v21\n"
        "v_fma_f32 v55, v55, v17, -v21\n"
        "v_mul_f32 v48, v48, v24\n"
        "v_mul_f32 v49, v49, v25\n"
        "v_mul_f32 v50, v50, v26\n"
        "v_mul_f32 v51, v51, v27\n"
        "v_mul_f32 v52, v52, v28\n"
        "buffer_load_dwordx4 v[204:207], v75, s[44:47], s62 offen\n"
        "v_mul_f32 v53, v53, v29\n"
        "v_mul_f32 v54, v54, v30\n"
        "v_mul_f32 v55, v55, v31\n"
        "v_cvt_pk_bf16_f32 v60, v48, v49\n"
        "v_cvt_pk_bf16_f32 v61, v50, v51\n"
        "v_cvt_pk_bf16_f32 v62, v52, v53\n"
        "v_cvt_pk_bf16_f32 v63, v54, v55\n"
        "ds_write_b128 v88, v[60:63] offset:64\n"
        "v_cvt_f32_i32 v40, v162\n"
        "v_cvt_f32_i32 v41, v166\n"
        "v_cvt_f32_i32 v42, v170\n"
        "v_cvt_f32_i32 v43, v174\n"
        "v_cvt_f32_i32 v44, v178\n"
        "v_cvt_f32_i32 v45, v182\n"
        "v_cvt_f32_i32 v46, v186\n"
        "buffer_load_dwordx4 v[208:211], v76, s[44:47], s62 offen\n"
        "v_cvt_f32_i32 v47, v190\n"
        "v_fma_f32 v40, v40, v18, -v22\n"
        "v_fma_f32 v41, v41, v18, -v22\n"
        "v_fma_f32 v42, v42, v18, -v22\n"
        "v_fma_f32 v43, v43, v18, -v22\n"
        "v_fma_f32 v44, v44, v18, -v22\n"
        "v_fma_f32 v45, v45, v18, -v22\n"
        "v_fma_f32 v46, v46, v18, -v22\n"
        "v_fma_f32 v47, v47, v18, -v22\n"
        "v_mul_f32 v40, v40, v24\n"
        "v_mul_f32 v41, v41, v25\n"
        "v_mul_f32 v42, v42, v26\n"
        "v_mul_f32 v43, v43, v27\n"
        "v_mul_f32 v44, v44, v28\n"
        "buffer_load_dwordx4 v[212:215], v77, s[44:47], s62 offen\n"
        "v_mul_f32 v45, v45, v29\n"
        "v_mul_f32 v46, v46, v30\n"
        "v_mul_f32 v47, v47, v31\n"
        "v_cvt_pk_bf16_f32 v64, v40, v41\n"
        "v_cvt_pk_bf16_f32 v65, v42, v43\n"
        "v_cvt_pk_bf16_f32 v66, v44, v45\n"
        "v_cvt_pk_bf16_f32 v67, v46, v47\n"
        "ds_write_b128 v88, v[64:67] offset:128\n"
        "v_cvt_f32_i32 v48, v163\n"
        "v_cvt_f32_i32 v49, v167\n"
        "v_cvt_f32_i32 v50, v171\n"
        "v_cvt_f32_i32 v51, v175\n"
        "v_cvt_f32_i32 v52, v179\n"
        "v_cvt_f32_i32 v53, v183\n"
        "v_cvt_f32_i32 v54, v187\n"
        "buffer_load_dwordx4 v[216:219], v78, s[44:47], s62 offen\n"
        "v_cvt_f32_i32 v55, v191\n"
        "v_fma_f32 v48, v48, v19, -v23\n"
        "v_fma_f32 v49, v49, v19, -v23\n"
        "v_fma_f32 v50, v50, v19, -v23\n"
        "v_fma_f32 v51, v51, v19, -v23\n"
        "v_fma_f32 v52, v52, v19, -v23\n"
        "v_fma_f32 v53, v53, v19, -v23\n"
        "v_fma_f32 v54, v54, v19, -v23\n"
        "v_fma_f32 v55, v55, v19, -v23\n"
        "v_mul_f32 v48, v48, v24\n"
        "v_mul_f32 v49, v49, v25\n"
        "v_mul_f32 v50, v50, v26\n"
        "v_mul_f32 v51, v51, v27\n"
        "v_mul_f32 v52, v52, v28\n"
        "buffer_load_dwordx4 v[220:223], v79, s[44:47], s62 offen\n"
        "v_mul_f32 v53, v53, v29\n"
        "v_mul_f32 v54, v54, v30\n"
        "v_mul_f32 v55, v55, v31\n"
        "v_cvt_pk_bf16_f32 v68, v48, v49\n"
        "v_cvt_pk_bf16_f32 v69, v50, v51\n"
        "v_cvt_pk_bf16_f32 v70, v52, v53\n"
        "v_cvt_pk_bf16_f32 v71, v54, v55\n"
        "ds_write_b128 v88, v[68:71] offset:192\n"
        "s_waitcnt lgkmcnt(0)\n"
        "s_barrier\n"
        "s_setprio 1\n"
        "ds_read_b128 v[32:35], v89 offset:512\n"
        "ds_read_b128 v[36:39], v89 offset:528\n"
        "s_waitcnt vmcnt(0)\n"
        "s_waitcnt lgkmcnt(0)\n"
        "v_cvt_f32_i32 v40, v192\n"
        "v_cvt_f32_i32 v41, v196\n"
        "v_cvt_f32_i32 v42, v200\n"
        "buffer_load_dwordx4 v[224:227], v72, s[44:47], s63 offen\n"
        "v_cvt_f32_i32 v43, v204\n"
        "v_cvt_f32_i32 v44, v208\n"
        "v_cvt_f32_i32 v45, v212\n"
        "v_cvt_f32_i32 v46, v216\n"
        "v_cvt_f32_i32 v47, v220\n"
        "v_fma_f32 v40, v40, v16, -v20\n"
        "v_fma_f32 v41, v41, v16, -v20\n"
        "buffer_load_dwordx4 v[228:231], v73, s[44:47], s63 offen\n"
        "v_fma_f32 v42, v42, v16, -v20\n"
        "v_fma_f32 v43, v43, v16, -v20\n"
        "v_fma_f32 v44, v44, v16, -v20\n"
        "v_fma_f32 v45, v45, v16, -v20\n"
        "v_fma_f32 v46, v46, v16, -v20\n"
        "v_fma_f32 v47, v47, v16, -v20\n"
        "v_mul_f32 v40, v40, v32\n"
        "buffer_load_dwordx4 v[232:235], v74, s[44:47], s63 offen\n"
        "v_mul_f32 v41, v41, v33\n"
        "v_mul_f32 v42, v42, v34\n"
        "v_mul_f32 v43, v43, v35\n"
        "v_mul_f32 v44, v44, v36\n"
        "v_mul_f32 v45, v45, v37\n"
        "v_mul_f32 v46, v46, v38\n"
        "v_mul_f32 v47, v47, v39\n"
        "buffer_load_dwordx4 v[236:239], v75, s[44:47], s63 offen\n"
        "v_cvt_pk_bf16_f32 v56, v40, v41\n"
        "v_cvt_pk_bf16_f32 v57, v42, v43\n"
        "v_cvt_pk_bf16_f32 v58, v44, v45\n"
        "v_cvt_pk_bf16_f32 v59, v46, v47\n"
        "ds_write_b128 v88, v[56:59] offset:34816\n"
        "ds_read_b128 v[24:27], v89 offset:768\n"
        "ds_read_b128 v[28:31], v89 offset:784\n"
        "v_cvt_f32_i32 v48, v193\n"
        "v_cvt_f32_i32 v49, v197\n"
        "v_cvt_f32_i32 v50, v201\n"
        "buffer_load_dwordx4 v[240:243], v76, s[44:47], s63 offen\n"
        "v_cvt_f32_i32 v51, v205\n"
        "v_cvt_f32_i32 v52, v209\n"
        "v_cvt_f32_i32 v53, v213\n"
        "v_cvt_f32_i32 v54, v217\n"
        "v_cvt_f32_i32 v55, v221\n"
        "v_fma_f32 v48, v48, v17, -v21\n"
        "v_fma_f32 v49, v49, v17, -v21\n"
        "buffer_load_dwordx4 v[244:247], v77, s[44:47], s63 offen\n"
        "v_fma_f32 v50, v50, v17, -v21\n"
        "v_fma_f32 v51, v51, v17, -v21\n"
        "v_fma_f32 v52, v52, v17, -v21\n"
        "v_fma_f32 v53, v53, v17, -v21\n"
        "v_fma_f32 v54, v54, v17, -v21\n"
        "v_fma_f32 v55, v55, v17, -v21\n"
        "v_mul_f32 v48, v48, v32\n"
        "buffer_load_dwordx4 v[248:251], v78, s[44:47], s63 offen\n"
        "v_mul_f32 v49, v49, v33\n"
        "v_mul_f32 v50, v50, v34\n"
        "v_mul_f32 v51, v51, v35\n"
        "v_mul_f32 v52, v52, v36\n"
        "v_mul_f32 v53, v53, v37\n"
        "v_mul_f32 v54, v54, v38\n"
        "v_mul_f32 v55, v55, v39\n"
        "buffer_load_dwordx4 v[252:255], v79, s[44:47], s63 offen\n"
        "v_cvt_pk_bf16_f32 v60, v48, v49\n"
        "v_cvt_pk_bf16_f32 v61, v50, v51\n"
        "v_cvt_pk_bf16_f32 v62, v52, v53\n"
        "v_cvt_pk_bf16_f32 v63, v54, v55\n"
        "ds_write_b128 v88, v[60:63] offset:34880\n"
        "v_cvt_f32_i32 v40, v194\n"
        "v_cvt_f32_i32 v41, v198\n"
        "v_cvt_f32_i32 v42, v202\n"
        "buffer_load_dwordx4 v[160:163], v72, s[44:47], s64 offen\n"
        "v_cvt_f32_i32 v43, v206\n"
        "v_cvt_f32_i32 v44, v210\n"
        "v_cvt_f32_i32 v45, v214\n"
        "v_cvt_f32_i32 v46, v218\n"
        "v_cvt_f32_i32 v47, v222\n"
        "v_fma_f32 v40, v40, v18, -v22\n"
        "v_fma_f32 v41, v41, v18, -v22\n"
        "buffer_load_dwordx4 v[164:167], v73, s[44:47], s64 offen\n"
        "v_fma_f32 v42, v42, v18, -v22\n"
        "v_fma_f32 v43, v43, v18, -v22\n"
        "v_fma_f32 v44, v44, v18, -v22\n"
        "v_fma_f32 v45, v45, v18, -v22\n"
        "v_fma_f32 v46, v46, v18, -v22\n"
        "v_fma_f32 v47, v47, v18, -v22\n"
        "v_mul_f32 v40, v40, v32\n"
        "buffer_load_dwordx4 v[168:171], v74, s[44:47], s64 offen\n"
        "v_mul_f32 v41, v41, v33\n"
        "v_mul_f32 v42, v42, v34\n"
        "v_mul_f32 v43, v43, v35\n"
        "v_mul_f32 v44, v44, v36\n"
        "v_mul_f32 v45, v45, v37\n"
        "v_mul_f32 v46, v46, v38\n"
        "v_mul_f32 v47, v47, v39\n"
        "buffer_load_dwordx4 v[172:175], v75, s[44:47], s64 offen\n"
        "v_cvt_pk_bf16_f32 v64, v40, v41\n"
        "v_cvt_pk_bf16_f32 v65, v42, v43\n"
        "v_cvt_pk_bf16_f32 v66, v44, v45\n"
        "v_cvt_pk_bf16_f32 v67, v46, v47\n"
        "ds_write_b128 v88, v[64:67] offset:34944\n"
        "v_cvt_f32_i32 v48, v195\n"
        "v_cvt_f32_i32 v49, v199\n"
        "v_cvt_f32_i32 v50, v203\n"
        "buffer_load_dwordx4 v[176:179], v76, s[44:47], s64 offen\n"
        "v_cvt_f32_i32 v51, v207\n"
        "v_cvt_f32_i32 v52, v211\n"
        "v_cvt_f32_i32 v53, v215\n"
        "v_cvt_f32_i32 v54, v219\n"
        "v_cvt_f32_i32 v55, v223\n"
        "v_fma_f32 v48, v48, v19, -v23\n"
        "v_fma_f32 v49, v49, v19, -v23\n"
        "buffer_load_dwordx4 v[180:183], v77, s[44:47], s64 offen\n"
        "v_fma_f32 v50, v50, v19, -v23\n"
        "v_fma_f32 v51, v51, v19, -v23\n"
        "v_fma_f32 v52, v52, v19, -v23\n"
        "v_fma_f32 v53, v53, v19, -v23\n"
        "v_fma_f32 v54, v54, v19, -v23\n"
        "v_fma_f32 v55, v55, v19, -v23\n"
        "v_mul_f32 v48, v48, v32\n"
        "buffer_load_dwordx4 v[184:187], v78, s[44:47], s64 offen\n"
        "v_mul_f32 v49, v49, v33\n"
        "v_mul_f32 v50, v50, v34\n"
        "v_mul_f32 v51, v51, v35\n"
        "v_mul_f32 v52, v52, v36\n"
        "v_mul_f32 v53, v53, v37\n"
        "v_mul_f32 v54, v54, v38\n"
        "v_mul_f32 v55, v55, v39\n"
        "buffer_load_dwordx4 v[188:191], v79, s[44:47], s64 offen\n"
        "v_cvt_pk_bf16_f32 v68, v48, v49\n"
        "v_cvt_pk_bf16_f32 v69, v50, v51\n"
        "v_cvt_pk_bf16_f32 v70, v52, v53\n"
        "v_cvt_pk_bf16_f32 v71, v54, v55\n"
        "ds_write_b128 v88, v[68:71] offset:35008\n"
        "s_barrier\n"
        "s_waitcnt vmcnt(8)\n"
        "s_waitcnt lgkmcnt(3)\n"
        "v_cvt_f32_i32 v40, v224\n"
        "v_cvt_f32_i32 v41, v228\n"
        "v_cvt_f32_i32 v42, v232\n"
        "v_cvt_f32_i32 v43, v236\n"
        "v_cvt_f32_i32 v44, v240\n"
        "v_cvt_f32_i32 v45, v244\n"
        "v_cvt_f32_i32 v46, v248\n"
        "buffer_load_dwordx4 v[192:195], v72, s[44:47], s65 offen\n"
        "v_cvt_f32_i32 v47, v252\n"
        "v_fma_f32 v40, v40, v16, -v20\n"
        "v_fma_f32 v41, v41, v16, -v20\n"
        "v_fma_f32 v42, v42, v16, -v20\n"
        "v_fma_f32 v43, v43, v16, -v20\n"
        "v_fma_f32 v44, v44, v16, -v20\n"
        "v_fma_f32 v45, v45, v16, -v20\n"
        "v_fma_f32 v46, v46, v16, -v20\n"
        "v_fma_f32 v47, v47, v16, -v20\n"
        "v_mul_f32 v40, v40, v24\n"
        "v_mul_f32 v41, v41, v25\n"
        "v_mul_f32 v42, v42, v26\n"
        "v_mul_f32 v43, v43, v27\n"
        "v_mul_f32 v44, v44, v28\n"
        "buffer_load_dwordx4 v[196:199], v73, s[44:47], s65 offen\n"
        "v_mul_f32 v45, v45, v29\n"
        "v_mul_f32 v46, v46, v30\n"
        "v_mul_f32 v47, v47, v31\n"
        "v_cvt_pk_bf16_f32 v56, v40, v41\n"
        "v_cvt_pk_bf16_f32 v57, v42, v43\n"
        "v_cvt_pk_bf16_f32 v58, v44, v45\n"
        "v_cvt_pk_bf16_f32 v59, v46, v47\n"
        "ds_write_b128 v88, v[56:59] offset:0\n"
        "ds_read_b128 v[32:35], v89 offset:1024\n"
        "ds_read_b128 v[36:39], v89 offset:1040\n"
        "v_cvt_f32_i32 v48, v225\n"
        "v_cvt_f32_i32 v49, v229\n"
        "v_cvt_f32_i32 v50, v233\n"
        "v_cvt_f32_i32 v51, v237\n"
        "v_cvt_f32_i32 v52, v241\n"
        "v_cvt_f32_i32 v53, v245\n"
        "v_cvt_f32_i32 v54, v249\n"
        "buffer_load_dwordx4 v[200:203], v74, s[44:47], s65 offen\n"
        "v_cvt_f32_i32 v55, v253\n"
        "v_fma_f32 v48, v48, v17, -v21\n"
        "v_fma_f32 v49, v49, v17, -v21\n"
        "v_fma_f32 v50, v50, v17, -v21\n"
        "v_fma_f32 v51, v51, v17, -v21\n"
        "v_fma_f32 v52, v52, v17, -v21\n"
        "v_fma_f32 v53, v53, v17, -v21\n"
        "v_fma_f32 v54, v54, v17, -v21\n"
        "v_fma_f32 v55, v55, v17, -v21\n"
        "v_mul_f32 v48, v48, v24\n"
        "v_mul_f32 v49, v49, v25\n"
        "v_mul_f32 v50, v50, v26\n"
        "v_mul_f32 v51, v51, v27\n"
        "v_mul_f32 v52, v52, v28\n"
        "buffer_load_dwordx4 v[204:207], v75, s[44:47], s65 offen\n"
        "v_mul_f32 v53, v53, v29\n"
        "v_mul_f32 v54, v54, v30\n"
        "v_mul_f32 v55, v55, v31\n"
        "v_cvt_pk_bf16_f32 v60, v48, v49\n"
        "v_cvt_pk_bf16_f32 v61, v50, v51\n"
        "v_cvt_pk_bf16_f32 v62, v52, v53\n"
        "v_cvt_pk_bf16_f32 v63, v54, v55\n"
        "ds_write_b128 v88, v[60:63] offset:64\n"
        "v_cvt_f32_i32 v40, v226\n"
        "v_cvt_f32_i32 v41, v230\n"
        "v_cvt_f32_i32 v42, v234\n"
        "v_cvt_f32_i32 v43, v238\n"
        "v_cvt_f32_i32 v44, v242\n"
        "v_cvt_f32_i32 v45, v246\n"
        "v_cvt_f32_i32 v46, v250\n"
        "buffer_load_dwordx4 v[208:211], v76, s[44:47], s65 offen\n"
        "v_cvt_f32_i32 v47, v254\n"
        "v_fma_f32 v40, v40, v18, -v22\n"
        "v_fma_f32 v41, v41, v18, -v22\n"
        "v_fma_f32 v42, v42, v18, -v22\n"
        "v_fma_f32 v43, v43, v18, -v22\n"
        "v_fma_f32 v44, v44, v18, -v22\n"
        "v_fma_f32 v45, v45, v18, -v22\n"
        "v_fma_f32 v46, v46, v18, -v22\n"
        "v_fma_f32 v47, v47, v18, -v22\n"
        "v_mul_f32 v40, v40, v24\n"
        "v_mul_f32 v41, v41, v25\n"
        "v_mul_f32 v42, v42, v26\n"
        "v_mul_f32 v43, v43, v27\n"
        "v_mul_f32 v44, v44, v28\n"
        "buffer_load_dwordx4 v[212:215], v77, s[44:47], s65 offen\n"
        "v_mul_f32 v45, v45, v29\n"
        "v_mul_f32 v46, v46, v30\n"
        "v_mul_f32 v47, v47, v31\n"
        "v_cvt_pk_bf16_f32 v64, v40, v41\n"
        "v_cvt_pk_bf16_f32 v65, v42, v43\n"
        "v_cvt_pk_bf16_f32 v66, v44, v45\n"
        "v_cvt_pk_bf16_f32 v67, v46, v47\n"
        "ds_write_b128 v88, v[64:67] offset:128\n"
        "v_cvt_f32_i32 v48, v227\n"
        "v_cvt_f32_i32 v49, v231\n"
        "v_cvt_f32_i32 v50, v235\n"
        "v_cvt_f32_i32 v51, v239\n"
        "v_cvt_f32_i32 v52, v243\n"
        "v_cvt_f32_i32 v53, v247\n"
        "v_cvt_f32_i32 v54, v251\n"
        "buffer_load_dwordx4 v[216:219], v78, s[44:47], s65 offen\n"
        "v_cvt_f32_i32 v55, v255\n"
        "v_fma_f32 v48, v48, v19, -v23\n"
        "v_fma_f32 v49, v49, v19, -v23\n"
        "v_fma_f32 v50, v50, v19, -v23\n"
        "v_fma_f32 v51, v51, v19, -v23\n"
        "v_fma_f32 v52, v52, v19, -v23\n"
        "v_fma_f32 v53, v53, v19, -v23\n"
        "v_fma_f32 v54, v54, v19, -v23\n"
        "v_fma_f32 v55, v55, v19, -v23\n"
        "v_mul_f32 v48, v48, v24\n"
        "v_mul_f32 v49, v49, v25\n"
        "v_mul_f32 v50, v50, v26\n"
        "v_mul_f32 v51, v51, v27\n"
        "v_mul_f32 v52, v52, v28\n"
        "buffer_load_dwordx4 v[220:223], v79, s[44:47], s65 offen\n"
        "v_mul_f32 v53, v53, v29\n"
        "v_mul_f32 v54, v54, v30\n"
        "v_mul_f32 v55, v55, v31\n"
        "v_cvt_pk_bf16_f32 v68, v48, v49\n"
        "v_cvt_pk_bf16_f32 v69, v50, v51\n"
        "v_cvt_pk_bf16_f32 v70, v52, v53\n"
        "v_cvt_pk_bf16_f32 v71, v54, v55\n"
        "ds_write_b128 v88, v[68:71] offset:192\n"
        "s_waitcnt lgkmcnt(6)\n"
        "s_barrier\n"
        "s_waitcnt vmcnt(8)\n"
        "s_waitcnt lgkmcnt(3)\n"
        "v_cvt_f32_i32 v40, v160\n"
        "v_cvt_f32_i32 v41, v164\n"
        "v_cvt_f32_i32 v42, v168\n"
        "v_cvt_f32_i32 v43, v172\n"
        "v_cvt_f32_i32 v44, v176\n"
        "v_cvt_f32_i32 v45, v180\n"
        "v_cvt_f32_i32 v46, v184\n"
        "buffer_load_dwordx4 v[224:227], v72, s[44:47], s66 offen\n"
        "v_cvt_f32_i32 v47, v188\n"
        "v_fma_f32 v40, v40, v16, -v20\n"
        "v_fma_f32 v41, v41, v16, -v20\n"
        "v_fma_f32 v42, v42, v16, -v20\n"
        "v_fma_f32 v43, v43, v16, -v20\n"
        "v_fma_f32 v44, v44, v16, -v20\n"
        "v_fma_f32 v45, v45, v16, -v20\n"
        "v_fma_f32 v46, v46, v16, -v20\n"
        "v_fma_f32 v47, v47, v16, -v20\n"
        "v_mul_f32 v40, v40, v32\n"
        "v_mul_f32 v41, v41, v33\n"
        "v_mul_f32 v42, v42, v34\n"
        "v_mul_f32 v43, v43, v35\n"
        "v_mul_f32 v44, v44, v36\n"
        "buffer_load_dwordx4 v[228:231], v73, s[44:47], s66 offen\n"
        "v_mul_f32 v45, v45, v37\n"
        "v_mul_f32 v46, v46, v38\n"
        "v_mul_f32 v47, v47, v39\n"
        "v_cvt_pk_bf16_f32 v56, v40, v41\n"
        "v_cvt_pk_bf16_f32 v57, v42, v43\n"
        "v_cvt_pk_bf16_f32 v58, v44, v45\n"
        "v_cvt_pk_bf16_f32 v59, v46, v47\n"
        "ds_write_b128 v88, v[56:59] offset:17408\n"
        "ds_read_b128 v[24:27], v89 offset:1280\n"
        "ds_read_b128 v[28:31], v89 offset:1296\n"
        "v_cvt_f32_i32 v48, v161\n"
        "v_cvt_f32_i32 v49, v165\n"
        "v_cvt_f32_i32 v50, v169\n"
        "v_cvt_f32_i32 v51, v173\n"
        "v_cvt_f32_i32 v52, v177\n"
        "v_cvt_f32_i32 v53, v181\n"
        "v_cvt_f32_i32 v54, v185\n"
        "buffer_load_dwordx4 v[232:235], v74, s[44:47], s66 offen\n"
        "v_cvt_f32_i32 v55, v189\n"
        "v_fma_f32 v48, v48, v17, -v21\n"
        "v_fma_f32 v49, v49, v17, -v21\n"
        "v_fma_f32 v50, v50, v17, -v21\n"
        "v_fma_f32 v51, v51, v17, -v21\n"
        "v_fma_f32 v52, v52, v17, -v21\n"
        "v_fma_f32 v53, v53, v17, -v21\n"
        "v_fma_f32 v54, v54, v17, -v21\n"
        "v_fma_f32 v55, v55, v17, -v21\n"
        "v_mul_f32 v48, v48, v32\n"
        "v_mul_f32 v49, v49, v33\n"
        "v_mul_f32 v50, v50, v34\n"
        "v_mul_f32 v51, v51, v35\n"
        "v_mul_f32 v52, v52, v36\n"
        "buffer_load_dwordx4 v[236:239], v75, s[44:47], s66 offen\n"
        "v_mul_f32 v53, v53, v37\n"
        "v_mul_f32 v54, v54, v38\n"
        "v_mul_f32 v55, v55, v39\n"
        "v_cvt_pk_bf16_f32 v60, v48, v49\n"
        "v_cvt_pk_bf16_f32 v61, v50, v51\n"
        "v_cvt_pk_bf16_f32 v62, v52, v53\n"
        "v_cvt_pk_bf16_f32 v63, v54, v55\n"
        "ds_write_b128 v88, v[60:63] offset:17472\n"
        "v_cvt_f32_i32 v40, v162\n"
        "v_cvt_f32_i32 v41, v166\n"
        "v_cvt_f32_i32 v42, v170\n"
        "v_cvt_f32_i32 v43, v174\n"
        "v_cvt_f32_i32 v44, v178\n"
        "v_cvt_f32_i32 v45, v182\n"
        "v_cvt_f32_i32 v46, v186\n"
        "buffer_load_dwordx4 v[240:243], v76, s[44:47], s66 offen\n"
        "v_cvt_f32_i32 v47, v190\n"
        "v_fma_f32 v40, v40, v18, -v22\n"
        "v_fma_f32 v41, v41, v18, -v22\n"
        "v_fma_f32 v42, v42, v18, -v22\n"
        "v_fma_f32 v43, v43, v18, -v22\n"
        "v_fma_f32 v44, v44, v18, -v22\n"
        "v_fma_f32 v45, v45, v18, -v22\n"
        "v_fma_f32 v46, v46, v18, -v22\n"
        "v_fma_f32 v47, v47, v18, -v22\n"
        "v_mul_f32 v40, v40, v32\n"
        "v_mul_f32 v41, v41, v33\n"
        "v_mul_f32 v42, v42, v34\n"
        "v_mul_f32 v43, v43, v35\n"
        "v_mul_f32 v44, v44, v36\n"
        "buffer_load_dwordx4 v[244:247], v77, s[44:47], s66 offen\n"
        "v_mul_f32 v45, v45, v37\n"
        "v_mul_f32 v46, v46, v38\n"
        "v_mul_f32 v47, v47, v39\n"
        "v_cvt_pk_bf16_f32 v64, v40, v41\n"
        "v_cvt_pk_bf16_f32 v65, v42, v43\n"
        "v_cvt_pk_bf16_f32 v66, v44, v45\n"
        "v_cvt_pk_bf16_f32 v67, v46, v47\n"
        "ds_write_b128 v88, v[64:67] offset:17536\n"
        "v_cvt_f32_i32 v48, v163\n"
        "v_cvt_f32_i32 v49, v167\n"
        "v_cvt_f32_i32 v50, v171\n"
        "v_cvt_f32_i32 v51, v175\n"
        "v_cvt_f32_i32 v52, v179\n"
        "v_cvt_f32_i32 v53, v183\n"
        "v_cvt_f32_i32 v54, v187\n"
        "buffer_load_dwordx4 v[248:251], v78, s[44:47], s66 offen\n"
        "v_cvt_f32_i32 v55, v191\n"
        "v_fma_f32 v48, v48, v19, -v23\n"
        "v_fma_f32 v49, v49, v19, -v23\n"
        "v_fma_f32 v50, v50, v19, -v23\n"
        "v_fma_f32 v51, v51, v19, -v23\n"
        "v_fma_f32 v52, v52, v19, -v23\n"
        "v_fma_f32 v53, v53, v19, -v23\n"
        "v_fma_f32 v54, v54, v19, -v23\n"
        "v_fma_f32 v55, v55, v19, -v23\n"
        "v_mul_f32 v48, v48, v32\n"
        "v_mul_f32 v49, v49, v33\n"
        "v_mul_f32 v50, v50, v34\n"
        "v_mul_f32 v51, v51, v35\n"
        "v_mul_f32 v52, v52, v36\n"
        "buffer_load_dwordx4 v[252:255], v79, s[44:47], s66 offen\n"
        "v_mul_f32 v53, v53, v37\n"
        "v_mul_f32 v54, v54, v38\n"
        "v_mul_f32 v55, v55, v39\n"
        "v_cvt_pk_bf16_f32 v68, v48, v49\n"
        "v_cvt_pk_bf16_f32 v69, v50, v51\n"
        "v_cvt_pk_bf16_f32 v70, v52, v53\n"
        "v_cvt_pk_bf16_f32 v71, v54, v55\n"
        "ds_write_b128 v88, v[68:71] offset:17600\n"
        "s_waitcnt lgkmcnt(6)\n"
        "s_barrier\n"
        "s_waitcnt vmcnt(8)\n"
        "s_waitcnt lgkmcnt(3)\n"
        "v_cvt_f32_i32 v40, v192\n"
        "v_cvt_f32_i32 v41, v196\n"
        "v_cvt_f32_i32 v42, v200\n"
        "v_cvt_f32_i32 v43, v204\n"
        "v_cvt_f32_i32 v44, v208\n"
        "v_cvt_f32_i32 v45, v212\n"
        "v_cvt_f32_i32 v46, v216\n"
        "buffer_load_dwordx4 v[160:163], v72, s[44:47], s67 offen\n"
        "v_cvt_f32_i32 v47, v220\n"
        "v_fma_f32 v40, v40, v16, -v20\n"
        "v_fma_f32 v41, v41, v16, -v20\n"
        "v_fma_f32 v42, v42, v16, -v20\n"
        "v_fma_f32 v43, v43, v16, -v20\n"
        "v_fma_f32 v44, v44, v16, -v20\n"
        "v_fma_f32 v45, v45, v16, -v20\n"
        "v_fma_f32 v46, v46, v16, -v20\n"
        "v_fma_f32 v47, v47, v16, -v20\n"
        "v_mul_f32 v40, v40, v24\n"
        "v_mul_f32 v41, v41, v25\n"
        "v_mul_f32 v42, v42, v26\n"
        "v_mul_f32 v43, v43, v27\n"
        "v_mul_f32 v44, v44, v28\n"
        "buffer_load_dwordx4 v[164:167], v73, s[44:47], s67 offen\n"
        "v_mul_f32 v45, v45, v29\n"
        "v_mul_f32 v46, v46, v30\n"
        "v_mul_f32 v47, v47, v31\n"
        "v_cvt_pk_bf16_f32 v56, v40, v41\n"
        "v_cvt_pk_bf16_f32 v57, v42, v43\n"
        "v_cvt_pk_bf16_f32 v58, v44, v45\n"
        "v_cvt_pk_bf16_f32 v59, v46, v47\n"
        "ds_write_b128 v88, v[56:59] offset:34816\n"
        "ds_read_b128 v[32:35], v89 offset:1536\n"
        "ds_read_b128 v[36:39], v89 offset:1552\n"
        "v_cvt_f32_i32 v48, v193\n"
        "v_cvt_f32_i32 v49, v197\n"
        "v_cvt_f32_i32 v50, v201\n"
        "v_cvt_f32_i32 v51, v205\n"
        "v_cvt_f32_i32 v52, v209\n"
        "v_cvt_f32_i32 v53, v213\n"
        "v_cvt_f32_i32 v54, v217\n"
        "buffer_load_dwordx4 v[168:171], v74, s[44:47], s67 offen\n"
        "v_cvt_f32_i32 v55, v221\n"
        "v_fma_f32 v48, v48, v17, -v21\n"
        "v_fma_f32 v49, v49, v17, -v21\n"
        "v_fma_f32 v50, v50, v17, -v21\n"
        "v_fma_f32 v51, v51, v17, -v21\n"
        "v_fma_f32 v52, v52, v17, -v21\n"
        "v_fma_f32 v53, v53, v17, -v21\n"
        "v_fma_f32 v54, v54, v17, -v21\n"
        "v_fma_f32 v55, v55, v17, -v21\n"
        "v_mul_f32 v48, v48, v24\n"
        "v_mul_f32 v49, v49, v25\n"
        "v_mul_f32 v50, v50, v26\n"
        "v_mul_f32 v51, v51, v27\n"
        "v_mul_f32 v52, v52, v28\n"
        "buffer_load_dwordx4 v[172:175], v75, s[44:47], s67 offen\n"
        "v_mul_f32 v53, v53, v29\n"
        "v_mul_f32 v54, v54, v30\n"
        "v_mul_f32 v55, v55, v31\n"
        "v_cvt_pk_bf16_f32 v60, v48, v49\n"
        "v_cvt_pk_bf16_f32 v61, v50, v51\n"
        "v_cvt_pk_bf16_f32 v62, v52, v53\n"
        "v_cvt_pk_bf16_f32 v63, v54, v55\n"
        "ds_write_b128 v88, v[60:63] offset:34880\n"
        "v_cvt_f32_i32 v40, v194\n"
        "v_cvt_f32_i32 v41, v198\n"
        "v_cvt_f32_i32 v42, v202\n"
        "v_cvt_f32_i32 v43, v206\n"
        "v_cvt_f32_i32 v44, v210\n"
        "v_cvt_f32_i32 v45, v214\n"
        "v_cvt_f32_i32 v46, v218\n"
        "buffer_load_dwordx4 v[176:179], v76, s[44:47], s67 offen\n"
        "v_cvt_f32_i32 v47, v222\n"
        "v_fma_f32 v40, v40, v18, -v22\n"
        "v_fma_f32 v41, v41, v18, -v22\n"
        "v_fma_f32 v42, v42, v18, -v22\n"
        "v_fma_f32 v43, v43, v18, -v22\n"
        "v_fma_f32 v44, v44, v18, -v22\n"
        "v_fma_f32 v45, v45, v18, -v22\n"
        "v_fma_f32 v46, v46, v18, -v22\n"
        "v_fma_f32 v47, v47, v18, -v22\n"
        "v_mul_f32 v40, v40, v24\n"
        "v_mul_f32 v41, v41, v25\n"
        "v_mul_f32 v42, v42, v26\n"
        "v_mul_f32 v43, v43, v27\n"
        "v_mul_f32 v44, v44, v28\n"
        "buffer_load_dwordx4 v[180:183], v77, s[44:47], s67 offen\n"
        "v_mul_f32 v45, v45, v29\n"
        "v_mul_f32 v46, v46, v30\n"
        "v_mul_f32 v47, v47, v31\n"
        "v_cvt_pk_bf16_f32 v64, v40, v41\n"
        "v_cvt_pk_bf16_f32 v65, v42, v43\n"
        "v_cvt_pk_bf16_f32 v66, v44, v45\n"
        "v_cvt_pk_bf16_f32 v67, v46, v47\n"
        "ds_write_b128 v88, v[64:67] offset:34944\n"
        "v_cvt_f32_i32 v48, v195\n"
        "v_cvt_f32_i32 v49, v199\n"
        "v_cvt_f32_i32 v50, v203\n"
        "v_cvt_f32_i32 v51, v207\n"
        "v_cvt_f32_i32 v52, v211\n"
        "v_cvt_f32_i32 v53, v215\n"
        "v_cvt_f32_i32 v54, v219\n"
        "buffer_load_dwordx4 v[184:187], v78, s[44:47], s67 offen\n"
        "v_cvt_f32_i32 v55, v223\n"
        "v_fma_f32 v48, v48, v19, -v23\n"
        "v_fma_f32 v49, v49, v19, -v23\n"
        "v_fma_f32 v50, v50, v19, -v23\n"
        "v_fma_f32 v51, v51, v19, -v23\n"
        "v_fma_f32 v52, v52, v19, -v23\n"
        "v_fma_f32 v53, v53, v19, -v23\n"
        "v_fma_f32 v54, v54, v19, -v23\n"
        "v_fma_f32 v55, v55, v19, -v23\n"
        "v_mul_f32 v48, v48, v24\n"
        "v_mul_f32 v49, v49, v25\n"
        "v_mul_f32 v50, v50, v26\n"
        "v_mul_f32 v51, v51, v27\n"
        "v_mul_f32 v52, v52, v28\n"
        "buffer_load_dwordx4 v[188:191], v79, s[44:47], s67 offen\n"
        "v_mul_f32 v53, v53, v29\n"
        "v_mul_f32 v54, v54, v30\n"
        "v_mul_f32 v55, v55, v31\n"
        "v_cvt_pk_bf16_f32 v68, v48, v49\n"
        "v_cvt_pk_bf16_f32 v69, v50, v51\n"
        "v_cvt_pk_bf16_f32 v70, v52, v53\n"
        "v_cvt_pk_bf16_f32 v71, v54, v55\n"
        "ds_write_b128 v88, v[68:71] offset:35008\n"
        "s_waitcnt lgkmcnt(6)\n"
        "s_barrier\n"
        "s_waitcnt vmcnt(8)\n"
        "s_waitcnt lgkmcnt(3)\n"
        "v_cvt_f32_i32 v40, v224\n"
        "v_cvt_f32_i32 v41, v228\n"
        "v_cvt_f32_i32 v42, v232\n"
        "v_cvt_f32_i32 v43, v236\n"
        "v_cvt_f32_i32 v44, v240\n"
        "v_cvt_f32_i32 v45, v244\n"
        "v_cvt_f32_i32 v46, v248\n"
        "buffer_load_dwordx4 v[192:195], v72, s[44:47], s68 offen\n"
        "v_cvt_f32_i32 v47, v252\n"
        "v_fma_f32 v40, v40, v16, -v20\n"
        "v_fma_f32 v41, v41, v16, -v20\n"
        "v_fma_f32 v42, v42, v16, -v20\n"
        "v_fma_f32 v43, v43, v16, -v20\n"
        "v_fma_f32 v44, v44, v16, -v20\n"
        "v_fma_f32 v45, v45, v16, -v20\n"
        "v_fma_f32 v46, v46, v16, -v20\n"
        "v_fma_f32 v47, v47, v16, -v20\n"
        "v_mul_f32 v40, v40, v32\n"
        "v_mul_f32 v41, v41, v33\n"
        "v_mul_f32 v42, v42, v34\n"
        "v_mul_f32 v43, v43, v35\n"
        "v_mul_f32 v44, v44, v36\n"
        "buffer_load_dwordx4 v[196:199], v73, s[44:47], s68 offen\n"
        "v_mul_f32 v45, v45, v37\n"
        "v_mul_f32 v46, v46, v38\n"
        "v_mul_f32 v47, v47, v39\n"
        "v_cvt_pk_bf16_f32 v56, v40, v41\n"
        "v_cvt_pk_bf16_f32 v57, v42, v43\n"
        "v_cvt_pk_bf16_f32 v58, v44, v45\n"
        "v_cvt_pk_bf16_f32 v59, v46, v47\n"
        "ds_write_b128 v88, v[56:59] offset:0\n"
        "ds_read_b128 v[24:27], v89 offset:1792\n"
        "ds_read_b128 v[28:31], v89 offset:1808\n"
        "v_cvt_f32_i32 v48, v225\n"
        "v_cvt_f32_i32 v49, v229\n"
        "v_cvt_f32_i32 v50, v233\n"
        "v_cvt_f32_i32 v51, v237\n"
        "v_cvt_f32_i32 v52, v241\n"
        "v_cvt_f32_i32 v53, v245\n"
        "v_cvt_f32_i32 v54, v249\n"
        "buffer_load_dwordx4 v[200:203], v74, s[44:47], s68 offen\n"
        "v_cvt_f32_i32 v55, v253\n"
        "v_fma_f32 v48, v48, v17, -v21\n"
        "v_fma_f32 v49, v49, v17, -v21\n"
        "v_fma_f32 v50, v50, v17, -v21\n"
        "v_fma_f32 v51, v51, v17, -v21\n"
        "v_fma_f32 v52, v52, v17, -v21\n"
        "v_fma_f32 v53, v53, v17, -v21\n"
        "v_fma_f32 v54, v54, v17, -v21\n"
        "v_fma_f32 v55, v55, v17, -v21\n"
        "v_mul_f32 v48, v48, v32\n"
        "v_mul_f32 v49, v49, v33\n"
        "v_mul_f32 v50, v50, v34\n"
        "v_mul_f32 v51, v51, v35\n"
        "v_mul_f32 v52, v52, v36\n"
        "buffer_load_dwordx4 v[204:207], v75, s[44:47], s68 offen\n"
        "v_mul_f32 v53, v53, v37\n"
        "v_mul_f32 v54, v54, v38\n"
        "v_mul_f32 v55, v55, v39\n"
        "v_cvt_pk_bf16_f32 v60, v48, v49\n"
        "v_cvt_pk_bf16_f32 v61, v50, v51\n"
        "v_cvt_pk_bf16_f32 v62, v52, v53\n"
        "v_cvt_pk_bf16_f32 v63, v54, v55\n"
        "ds_write_b128 v88, v[60:63] offset:64\n"
        "v_cvt_f32_i32 v40, v226\n"
        "v_cvt_f32_i32 v41, v230\n"
        "v_cvt_f32_i32 v42, v234\n"
        "v_cvt_f32_i32 v43, v238\n"
        "v_cvt_f32_i32 v44, v242\n"
        "v_cvt_f32_i32 v45, v246\n"
        "v_cvt_f32_i32 v46, v250\n"
        "buffer_load_dwordx4 v[208:211], v76, s[44:47], s68 offen\n"
        "v_cvt_f32_i32 v47, v254\n"
        "v_fma_f32 v40, v40, v18, -v22\n"
        "v_fma_f32 v41, v41, v18, -v22\n"
        "v_fma_f32 v42, v42, v18, -v22\n"
        "v_fma_f32 v43, v43, v18, -v22\n"
        "v_fma_f32 v44, v44, v18, -v22\n"
        "v_fma_f32 v45, v45, v18, -v22\n"
        "v_fma_f32 v46, v46, v18, -v22\n"
        "v_fma_f32 v47, v47, v18, -v22\n"
        "v_mul_f32 v40, v40, v32\n"
        "v_mul_f32 v41, v41, v33\n"
        "v_mul_f32 v42, v42, v34\n"
        "v_mul_f32 v43, v43, v35\n"
        "v_mul_f32 v44, v44, v36\n"
        "buffer_load_dwordx4 v[212:215], v77, s[44:47], s68 offen\n"
        "v_mul_f32 v45, v45, v37\n"
        "v_mul_f32 v46, v46, v38\n"
        "v_mul_f32 v47, v47, v39\n"
        "v_cvt_pk_bf16_f32 v64, v40, v41\n"
        "v_cvt_pk_bf16_f32 v65, v42, v43\n"
        "v_cvt_pk_bf16_f32 v66, v44, v45\n"
        "v_cvt_pk_bf16_f32 v67, v46, v47\n"
        "ds_write_b128 v88, v[64:67] offset:128\n"
        "v_cvt_f32_i32 v48, v227\n"
        "v_cvt_f32_i32 v49, v231\n"
        "v_cvt_f32_i32 v50, v235\n"
        "v_cvt_f32_i32 v51, v239\n"
        "v_cvt_f32_i32 v52, v243\n"
        "v_cvt_f32_i32 v53, v247\n"
        "v_cvt_f32_i32 v54, v251\n"
        "buffer_load_dwordx4 v[216:219], v78, s[44:47], s68 offen\n"
        "v_cvt_f32_i32 v55, v255\n"
        "v_fma_f32 v48, v48, v19, -v23\n"
        "v_fma_f32 v49, v49, v19, -v23\n"
        "v_fma_f32 v50, v50, v19, -v23\n"
        "v_fma_f32 v51, v51, v19, -v23\n"
        "v_fma_f32 v52, v52, v19, -v23\n"
        "v_fma_f32 v53, v53, v19, -v23\n"
        "v_fma_f32 v54, v54, v19, -v23\n"
        "v_fma_f32 v55, v55, v19, -v23\n"
        "v_mul_f32 v48, v48, v32\n"
        "v_mul_f32 v49, v49, v33\n"
        "v_mul_f32 v50, v50, v34\n"
        "v_mul_f32 v51, v51, v35\n"
        "v_mul_f32 v52, v52, v36\n"
        "buffer_load_dwordx4 v[220:223], v79, s[44:47], s68 offen\n"
        "v_mul_f32 v53, v53, v37\n"
        "v_mul_f32 v54, v54, v38\n"
        "v_mul_f32 v55, v55, v39\n"
        "v_cvt_pk_bf16_f32 v68, v48, v49\n"
        "v_cvt_pk_bf16_f32 v69, v50, v51\n"
        "v_cvt_pk_bf16_f32 v70, v52, v53\n"
        "v_cvt_pk_bf16_f32 v71, v54, v55\n"
        "ds_write_b128 v88, v[68:71] offset:192\n"
        "s_waitcnt lgkmcnt(6)\n"
        "s_barrier\n"
        "s_cmp_ge_u32 s82, 2\n"
        "s_cbranch_scc1 L_ex_%=\n"
        "s_and_b32 s78, s82, 1\n"
        "s_lshl_b32 s79, s78, 3\n"
        "v_lshrrev_b32 v12, 3, v2\n"
        "v_add_u32 v12, s79, v12\n"
        "v_bfe_u32 v13, v12, 1, 3\n"
        "v_and_b32 v14, 7, v2\n"
        "v_xor_b32 v13, v13, v14\n"
        "s_movk_i32 s80, 1152\n"
        "v_mul_lo_u32 v12, v12, s80\n"
        "v_lshl_add_u32 v90, v13, 4, v12\n"
        "s_lshl_b32 s87, s78, 10\n"
        "s_add_u32 s87, s87, s83\n"
        "s_mov_b32 s88, s12\n"
        "s_and_b32 s89, s13, 0xffff\n"
        "s_mov_b32 s90, 2359296\n"
        "s_mov_b32 s91, 0x20000\n"
        "s_waitcnt vmcnt(8)\n"
        "s_waitcnt lgkmcnt(3)\n"
        "v_cvt_f32_i32 v40, v160\n"
        "v_cvt_f32_i32 v41, v164\n"
        "v_cvt_f32_i32 v42, v168\n"
        "v_cvt_f32_i32 v43, v172\n"
        "v_cvt_f32_i32 v44, v176\n"
        "v_cvt_f32_i32 v45, v180\n"
        "v_cvt_f32_i32 v46, v184\n"
        "s_add_u32 s86, s39, 0\n"
        "s_sub_u32 s78, s86, 11\n"
        "s_min_u32 s86, s86, s78\n"
        "s_add_u32 s86, s86, s27\n"
        "s_add_u32 s86, s86, 8\n"
        "s_mul_i32 s86, s86, 18432\n"
        "s_add_u32 s85, s86, s69\n"
        "s_add_u32 m0, s87, 103680\n"
        "s_nop 0\n"
        "buffer_load_dwordx4 v90, s[88:91], s85 offen lds\n"
        "v_cvt_f32_i32 v47, v188\n"
        "v_fma_f32 v40, v40, v16, -v20\n"
        "v_fma_f32 v41, v41, v16, -v20\n"
        "v_fma_f32 v42, v42, v16, -v20\n"
        "v_fma_f32 v43, v43, v16, -v20\n"
        "v_fma_f32 v44, v44, v16, -v20\n"
        "v_fma_f32 v45, v45, v16, -v20\n"
        "v_fma_f32 v46, v46, v16, -v20\n"
        "v_fma_f32 v47, v47, v16, -v20\n"
        "v_mul_f32 v40, v40, v24\n"
        "v_mul_f32 v41, v41, v25\n"
        "v_mul_f32 v42, v42, v26\n"
        "v_mul_f32 v43, v43, v27\n"
        "v_mul_f32 v44, v44, v28\n"
        "s_add_u32 s85, s86, s70\n"
        "s_add_u32 m0, s87, 105728\n"
        "s_nop 0\n"
        "buffer_load_dwordx4 v90, s[88:91], s85 offen lds\n"
        "v_mul_f32 v45, v45, v29\n"
        "v_mul_f32 v46, v46, v30\n"
        "v_mul_f32 v47, v47, v31\n"
        "v_cvt_pk_bf16_f32 v56, v40, v41\n"
        "v_cvt_pk_bf16_f32 v57, v42, v43\n"
        "v_cvt_pk_bf16_f32 v58, v44, v45\n"
        "v_cvt_pk_bf16_f32 v59, v46, v47\n"
        "ds_write_b128 v88, v[56:59] offset:17408\n"
        "ds_read_b128 v[32:35], v89 offset:2048\n"
        "ds_read_b128 v[36:39], v89 offset:2064\n"
        "v_cvt_f32_i32 v48, v161\n"
        "v_cvt_f32_i32 v49, v165\n"
        "v_cvt_f32_i32 v50, v169\n"
        "v_cvt_f32_i32 v51, v173\n"
        "v_cvt_f32_i32 v52, v177\n"
        "v_cvt_f32_i32 v53, v181\n"
        "v_cvt_f32_i32 v54, v185\n"
        "s_add_u32 s85, s86, s71\n"
        "s_add_u32 m0, s87, 107776\n"
        "s_nop 0\n"
        "buffer_load_dwordx4 v90, s[88:91], s85 offen lds\n"
        "v_cvt_f32_i32 v55, v189\n"
        "v_fma_f32 v48, v48, v17, -v21\n"
        "v_fma_f32 v49, v49, v17, -v21\n"
        "v_fma_f32 v50, v50, v17, -v21\n"
        "v_fma_f32 v51, v51, v17, -v21\n"
        "v_fma_f32 v52, v52, v17, -v21\n"
        "v_fma_f32 v53, v53, v17, -v21\n"
        "v_fma_f32 v54, v54, v17, -v21\n"
        "v_fma_f32 v55, v55, v17, -v21\n"
        "v_mul_f32 v48, v48, v24\n"
        "v_mul_f32 v49, v49, v25\n"
        "v_mul_f32 v50, v50, v26\n"
        "v_mul_f32 v51, v51, v27\n"
        "v_mul_f32 v52, v52, v28\n"
        "s_add_u32 s85, s86, s72\n"
        "s_add_u32 m0, s87, 109824\n"
        "s_nop 0\n"
        "buffer_load_dwordx4 v90, s[88:91], s85 offen lds\n"
        "v_mul_f32 v53, v53, v29\n"
        "v_mul_f32 v54, v54, v30\n"
        "v_mul_f32 v55, v55, v31\n"
        "v_cvt_pk_bf16_f32 v60, v48, v49\n"
        "v_cvt_pk_bf16_f32 v61, v50, v51\n"
        "v_cvt_pk_bf16_f32 v62, v52, v53\n"
        "v_cvt_pk_bf16_f32 v63, v54, v55\n"
        "ds_write_b128 v88, v[60:63] offset:17472\n"
        "v_cvt_f32_i32 v40, v162\n"
        "v_cvt_f32_i32 v41, v166\n"
        "v_cvt_f32_i32 v42, v170\n"
        "v_cvt_f32_i32 v43, v174\n"
        "v_cvt_f32_i32 v44, v178\n"
        "v_cvt_f32_i32 v45, v182\n"
        "v_cvt_f32_i32 v46, v186\n"
        "s_add_u32 s85, s86, s73\n"
        "s_add_u32 m0, s87, 111872\n"
        "s_nop 0\n"
        "buffer_load_dwordx4 v90, s[88:91], s85 offen lds\n"
        "v_cvt_f32_i32 v47, v190\n"
        "v_fma_f32 v40, v40, v18, -v22\n"
        "v_fma_f32 v41, v41, v18, -v22\n"
        "v_fma_f32 v42, v42, v18, -v22\n"
        "v_fma_f32 v43, v43, v18, -v22\n"
        "v_fma_f32 v44, v44, v18, -v22\n"
        "v_fma_f32 v45, v45, v18, -v22\n"
        "v_fma_f32 v46, v46, v18, -v22\n"
        "v_fma_f32 v47, v47, v18, -v22\n"
        "v_mul_f32 v40, v40, v24\n"
        "v_mul_f32 v41, v41, v25\n"
        "v_mul_f32 v42, v42, v26\n"
        "v_mul_f32 v43, v43, v27\n"
        "v_mul_f32 v44, v44, v28\n"
        "s_add_u32 s85, s86, s74\n"
        "s_add_u32 m0, s87, 113920\n"
        "s_nop 0\n"
        "buffer_load_dwordx4 v90, s[88:91], s85 offen lds\n"
        "v_mul_f32 v45, v45, v29\n"
        "v_mul_f32 v46, v46, v30\n"
        "v_mul_f32 v47, v47, v31\n"
        "v_cvt_pk_bf16_f32 v64, v40, v41\n"
        "v_cvt_pk_bf16_f32 v65, v42, v43\n"
        "v_cvt_pk_bf16_f32 v66, v44, v45\n"
        "v_cvt_pk_bf16_f32 v67, v46, v47\n"
        "ds_write_b128 v88, v[64:67] offset:17536\n"
        "v_cvt_f32_i32 v48, v163\n"
        "v_cvt_f32_i32 v49, v167\n"
        "v_cvt_f32_i32 v50, v171\n"
        "v_cvt_f32_i32 v51, v175\n"
        "s_add_u32 s85, s86, s75\n"
        "s_add_u32 m0, s87, 115968\n"
        "s_nop 0\n"
        "buffer_load_dwordx4 v90, s[88:91], s85 offen lds\n"
        "v_cvt_f32_i32 v52, v179\n"
        "v_cvt_f32_i32 v53, v183\n"
        "v_cvt_f32_i32 v54, v187\n"
        "v_cvt_f32_i32 v55, v191\n"
        "v_fma_f32 v48, v48, v19, -v23\n"
        "v_fma_f32 v49, v49, v19, -v23\n"
        "v_fma_f32 v50, v50, v19, -v23\n"
        "v_fma_f32 v51, v51, v19, -v23\n"
        "v_fma_f32 v52, v52, v19, -v23\n"
        "v_fma_f32 v53, v53, v19, -v23\n"
        "s_add_u32 s85, s86, s76\n"
        "s_add_u32 m0, s87, 118016\n"
        "s_nop 0\n"
        "buffer_load_dwordx4 v90, s[88:91], s85 offen lds\n"
        "v_fma_f32 v54, v54, v19, -v23\n"
        "v_fma_f32 v55, v55, v19, -v23\n"
        "v_mul_f32 v48, v48, v24\n"
        "v_mul_f32 v49, v49, v25\n"
        "v_mul_f32 v50, v50, v26\n"
        "v_mul_f32 v51, v51, v27\n"
        "v_mul_f32 v52, v52, v28\n"
        "v_mul_f32 v53, v53, v29\n"
        "v_mul_f32 v54, v54, v30\n"
        "s_add_u32 s85, s86, s77\n"
        "s_add_u32 m0, s87, 120064\n"
        "s_nop 0\n"
        "buffer_load_dwordx4 v90, s[88:91], s85 offen lds\n"
        "v_mul_f32 v55, v55, v31\n"
        "v_cvt_pk_bf16_f32 v68, v48, v49\n"
        "v_cvt_pk_bf16_f32 v69, v50, v51\n"
        "v_cvt_pk_bf16_f32 v70, v52, v53\n"
        "v_cvt_pk_bf16_f32 v71, v54, v55\n"
        "ds_write_b128 v88, v[68:71] offset:17600\n"
        "s_waitcnt lgkmcnt(6)\n"
        "s_barrier\n"
        "s_waitcnt vmcnt(9)\n"
        "s_waitcnt lgkmcnt(3)\n"
        "v_cvt_f32_i32 v40, v192\n"
        "v_cvt_f32_i32 v41, v196\n"
        "v_cvt_f32_i32 v42, v200\n"
        "v_cvt_f32_i32 v43, v204\n"
        "v_cvt_f32_i32 v44, v208\n"
        "v_cvt_f32_i32 v45, v212\n"
        "v_cvt_f32_i32 v46, v216\n"
        "s_add_u32 s86, s39, 1\n"
        "s_sub_u32 s78, s86, 11\n"
        "s_min_u32 s86, s86, s78\n"
        "s_add_u32 s86, s86, s27\n"
        "s_add_u32 s86, s86, 8\n"
        "s_mul_i32 s86, s86, 18432\n"
        "s_add_u32 s85, s86, s69\n"
        "s_add_u32 m0, s87, 122112\n"
        "s_nop 0\n"
        "buffer_load_dwordx4 v90, s[88:91], s85 offen lds\n"
        "v_cvt_f32_i32 v47, v220\n"
        "v_fma_f32 v40, v40, v16, -v20\n"
        "v_fma_f32 v41, v41, v16, -v20\n"
        "v_fma_f32 v42, v42, v16, -v20\n"
        "v_fma_f32 v43, v43, v16, -v20\n"
        "v_fma_f32 v44, v44, v16, -v20\n"
        "v_fma_f32 v45, v45, v16, -v20\n"
        "v_fma_f32 v46, v46, v16, -v20\n"
        "v_fma_f32 v47, v47, v16, -v20\n"
        "v_mul_f32 v40, v40, v32\n"
        "v_mul_f32 v41, v41, v33\n"
        "v_mul_f32 v42, v42, v34\n"
        "v_mul_f32 v43, v43, v35\n"
        "v_mul_f32 v44, v44, v36\n"
        "s_add_u32 s85, s86, s70\n"
        "s_add_u32 m0, s87, 124160\n"
        "s_nop 0\n"
        "buffer_load_dwordx4 v90, s[88:91], s85 offen lds\n"
        "v_mul_f32 v45, v45, v37\n"
        "v_mul_f32 v46, v46, v38\n"
        "v_mul_f32 v47, v47, v39\n"
        "v_cvt_pk_bf16_f32 v56, v40, v41\n"
        "v_cvt_pk_bf16_f32 v57, v42, v43\n"
        "v_cvt_pk_bf16_f32 v58, v44, v45\n"
        "v_cvt_pk_bf16_f32 v59, v46, v47\n"
        "ds_write_b128 v88, v[56:59] offset:34816\n"
        "v_cvt_f32_i32 v48, v193\n"
        "v_cvt_f32_i32 v49, v197\n"
        "v_cvt_f32_i32 v50, v201\n"
        "v_cvt_f32_i32 v51, v205\n"
        "v_cvt_f32_i32 v52, v209\n"
        "v_cvt_f32_i32 v53, v213\n"
        "v_cvt_f32_i32 v54, v217\n"
        "s_add_u32 s85, s86, s71\n"
        "s_add_u32 m0, s87, 126208\n"
        "s_nop 0\n"
        "buffer_load_dwordx4 v90, s[88:91], s85 offen lds\n"
        "v_cvt_f32_i32 v55, v221\n"
        "v_fma_f32 v48, v48, v17, -v21\n"
        "v_fma_f32 v49, v49, v17, -v21\n"
        "v_fma_f32 v50, v50, v17, -v21\n"
        "v_fma_f32 v51, v51, v17, -v21\n"
        "v_fma_f32 v52, v52, v17, -v21\n"
        "v_fma_f32 v53, v53, v17, -v21\n"
        "v_fma_f32 v54, v54, v17, -v21\n"
        "v_fma_f32 v55, v55, v17, -v21\n"
        "v_mul_f32 v48, v48, v32\n"
        "v_mul_f32 v49, v49, v33\n"
        "v_mul_f32 v50, v50, v34\n"
        "v_mul_f32 v51, v51, v35\n"
        "v_mul_f32 v52, v52, v36\n"
        "s_add_u32 s85, s86, s72\n"
        "s_add_u32 m0, s87, 128256\n"
        "s_nop 0\n"
        "buffer_load_dwordx4 v90, s[88:91], s85 offen lds\n"
        "v_mul_f32 v53, v53, v37\n"
        "v_mul_f32 v54, v54, v38\n"
        "v_mul_f32 v55, v55, v39\n"
        "v_cvt_pk_bf16_f32 v60, v48, v49\n"
        "v_cvt_pk_bf16_f32 v61, v50, v51\n"
        "v_cvt_pk_bf16_f32 v62, v52, v53\n"
        "v_cvt_pk_bf16_f32 v63, v54, v55\n"
        "ds_write_b128 v88, v[60:63] offset:34880\n"
        "v_cvt_f32_i32 v40, v194\n"
        "v_cvt_f32_i32 v41, v198\n"
        "v_cvt_f32_i32 v42, v202\n"
        "v_cvt_f32_i32 v43, v206\n"
        "v_cvt_f32_i32 v44, v210\n"
        "v_cvt_f32_i32 v45, v214\n"
        "v_cvt_f32_i32 v46, v218\n"
        "s_add_u32 s85, s86, s73\n"
        "s_add_u32 m0, s87, 130304\n"
        "s_nop 0\n"
        "buffer_load_dwordx4 v90, s[88:91], s85 offen lds\n"
        "v_cvt_f32_i32 v47, v222\n"
        "v_fma_f32 v40, v40, v18, -v22\n"
        "v_fma_f32 v41, v41, v18, -v22\n"
        "v_fma_f32 v42, v42, v18, -v22\n"
        "v_fma_f32 v43, v43, v18, -v22\n"
        "v_fma_f32 v44, v44, v18, -v22\n"
        "v_fma_f32 v45, v45, v18, -v22\n"
        "v_fma_f32 v46, v46, v18, -v22\n"
        "v_fma_f32 v47, v47, v18, -v22\n"
        "v_mul_f32 v40, v40, v32\n"
        "v_mul_f32 v41, v41, v33\n"
        "v_mul_f32 v42, v42, v34\n"
        "v_mul_f32 v43, v43, v35\n"
        "v_mul_f32 v44, v44, v36\n"
        "s_add_u32 s85, s86, s74\n"
        "s_add_u32 m0, s87, 132352\n"
        "s_nop 0\n"
        "buffer_load_dwordx4 v90, s[88:91], s85 offen lds\n"
        "v_mul_f32 v45, v45, v37\n"
        "v_mul_f32 v46, v46, v38\n"
        "v_mul_f32 v47, v47, v39\n"
        "v_cvt_pk_bf16_f32 v64, v40, v41\n"
        "v_cvt_pk_bf16_f32 v65, v42, v43\n"
        "v_cvt_pk_bf16_f32 v66, v44, v45\n"
        "v_cvt_pk_bf16_f32 v67, v46, v47\n"
        "ds_write_b128 v88, v[64:67] offset:34944\n"
        "v_cvt_f32_i32 v48, v195\n"
        "v_cvt_f32_i32 v49, v199\n"
        "v_cvt_f32_i32 v50, v203\n"
        "v_cvt_f32_i32 v51, v207\n"
        "s_add_u32 s85, s86, s75\n"
        "s_add_u32 m0, s87, 134400\n"
        "s_nop 0\n"
        "buffer_load_dwordx4 v90, s[88:91], s85 offen lds\n"
        "v_cvt_f32_i32 v52, v211\n"
        "v_cvt_f32_i32 v53, v215\n"
        "v_cvt_f32_i32 v54, v219\n"
        "v_cvt_f32_i32 v55, v223\n"
        "v_fma_f32 v48, v48, v19, -v23\n"
        "v_fma_f32 v49, v49, v19, -v23\n"
        "v_fma_f32 v50, v50, v19, -v23\n"
        "v_fma_f32 v51, v51, v19, -v23\n"
        "v_fma_f32 v52, v52, v19, -v23\n"
        "v_fma_f32 v53, v53, v19, -v23\n"
        "s_add_u32 s85, s86, s76\n"
        "s_add_u32 m0, s87, 136448\n"
        "s_nop 0\n"
        "buffer_load_dwordx4 v90, s[88:91], s85 offen lds\n"
        "v_fma_f32 v54, v54, v19, -v23\n"
        "v_fma_f32 v55, v55, v19, -v23\n"
        "v_mul_f32 v48, v48, v32\n"
        "v_mul_f32 v49, v49, v33\n"
        "v_mul_f32 v50, v50, v34\n"
        "v_mul_f32 v51, v51, v35\n"
        "v_mul_f32 v52, v52, v36\n"
        "v_mul_f32 v53, v53, v37\n"
        "v_mul_f32 v54, v54, v38\n"
        "s_add_u32 s85, s86, s77\n"
        "s_add_u32 m0, s87, 138496\n"
        "s_nop 0\n"
        "buffer_load_dwordx4 v90, s[88:91], s85 offen lds\n"
        "v_mul_f32 v55, v55, v39\n"
        "v_cvt_pk_bf16_f32 v68, v48, v49\n"
        "v_cvt_pk_bf16_f32 v69, v50, v51\n"
        "v_cvt_pk_bf16_f32 v70, v52, v53\n"
        "v_cvt_pk_bf16_f32 v71, v54, v55\n"
        "ds_write_b128 v88, v[68:71] offset:35008\n"
        "s_waitcnt lgkmcnt(4)\n"
        "s_barrier\n"
        "s_add_u32 s86, s39, 2\n"
        "s_sub_u32 s78, s86, 11\n"
        "s_min_u32 s86, s86, s78\n"
        "s_add_u32 s86, s86, s27\n"
        "s_add_u32 s86, s86, 8\n"
        "s_mul_i32 s86, s86, 18432\n"
        "s_add_u32 s85, s86, s69\n"
        "s_add_u32 m0, s87, 140544\n"
        "s_nop 0\n"
        "buffer_load_dwordx4 v90, s[88:91], s85 offen lds\n"
        "s_add_u32 s85, s86, s70\n"
        "s_add_u32 m0, s87, 142592\n"
        "s_nop 0\n"
        "buffer_load_dwordx4 v90, s[88:91], s85 offen lds\n"
        "s_add_u32 s85, s86, s71\n"
        "s_add_u32 m0, s87, 144640\n"
        "s_nop 0\n"
        "buffer_load_dwordx4 v90, s[88:91], s85 offen lds\n"
        "s_add_u32 s85, s86, s72\n"
        "s_add_u32 m0, s87, 146688\n"
        "s_nop 0\n"
        "buffer_load_dwordx4 v90, s[88:91], s85 offen lds\n"
        "s_add_u32 s85, s86, s73\n"
        "s_add_u32 m0, s87, 148736\n"
        "s_nop 0\n"
        "buffer_load_dwordx4 v90, s[88:91], s85 offen lds\n"
        "s_add_u32 s85, s86, s74\n"
        "s_add_u32 m0, s87, 150784\n"
        "s_nop 0\n"
        "buffer_load_dwordx4 v90, s[88:91], s85 offen lds\n"
        "s_add_u32 s85, s86, s75\n"
        "s_add_u32 m0, s87, 152832\n"
        "s_nop 0\n"
        "buffer_load_dwordx4 v90, s[88:91], s85 offen lds\n"
        "s_add_u32 s85, s86, s76\n"
        "s_add_u32 m0, s87, 154880\n"
        "s_nop 0\n"
        "buffer_load_dwordx4 v90, s[88:91], s85 offen lds\n"
        "s_add_u32 s85, s86, s77\n"
        "s_add_u32 m0, s87, 156928\n"
        "s_nop 0\n"
        "buffer_load_dwordx4 v90, s[88:91], s85 offen lds\n"
        "s_waitcnt lgkmcnt(0)\n"
        "s_waitcnt vmcnt(18)\n"
        "s_barrier\n"
        "s_add_u32 s86, s39, 3\n"
        "s_sub_u32 s78, s86, 11\n"
        "s_min_u32 s86, s86, s78\n"
        "s_add_u32 s86, s86, s27\n"
        "s_add_u32 s86, s86, 8\n"
        "s_mul_i32 s86, s86, 18432\n"
        "s_add_u32 s85, s86, s69\n"
        "s_add_u32 m0, s87, 0\n"
        "s_nop 0\n"
        "buffer_load_dwordx4 v90, s[88:91], s85 offen lds\n"
        "s_add_u32 s85, s86, s70\n"
        "s_add_u32 m0, s87, 2048\n"
        "s_nop 0\n"
        "buffer_load_dwordx4 v90, s[88:91], s85 offen lds\n"
        "s_add_u32 s85, s86, s71\n"
        "s_add_u32 m0, s87, 4096\n"
        "s_nop 0\n"
        "buffer_load_dwordx4 v90, s[88:91], s85 offen lds\n"
        "s_add_u32 s85, s86, s72\n"
        "s_add_u32 m0, s87, 6144\n"
        "s_nop 0\n"
        "buffer_load_dwordx4 v90, s[88:91], s85 offen lds\n"
        "s_add_u32 s85, s86, s73\n"
        "s_add_u32 m0, s87, 8192\n"
        "s_nop 0\n"
        "buffer_load_dwordx4 v90, s[88:91], s85 offen lds\n"
        "s_add_u32 s85, s86, s74\n"
        "s_add_u32 m0, s87, 10240\n"
        "s_nop 0\n"
        "buffer_load_dwordx4 v90, s[88:91], s85 offen lds\n"
        "s_add_u32 s85, s86, s75\n"
        "s_add_u32 m0, s87, 12288\n"
        "s_nop 0\n"
        "buffer_load_dwordx4 v90, s[88:91], s85 offen lds\n"
        "s_add_u32 s85, s86, s76\n"
        "s_add_u32 m0, s87, 14336\n"
        "s_nop 0\n"
        "buffer_load_dwordx4 v90, s[88:91], s85 offen lds\n"
        "s_add_u32 s85, s86, s77\n"
        "s_add_u32 m0, s87, 16384\n"
        "s_nop 0\n"
        "buffer_load_dwordx4 v90, s[88:91], s85 offen lds\n"
        "s_waitcnt vmcnt(18)\n"
        "s_barrier\n"
        "s_add_u32 s86, s39, 4\n"
        "s_sub_u32 s78, s86, 11\n"
        "s_min_u32 s86, s86, s78\n"
        "s_add_u32 s86, s86, s27\n"
        "s_add_u32 s86, s86, 8\n"
        "s_mul_i32 s86, s86, 18432\n"
        "s_add_u32 s85, s86, s69\n"
        "s_add_u32 m0, s87, 18432\n"
        "s_nop 0\n"
        "buffer_load_dwordx4 v90, s[88:91], s85 offen lds\n"
        "s_add_u32 s85, s86, s70\n"
        "s_add_u32 m0, s87, 20480\n"
        "s_nop 0\n"
        "buffer_load_dwordx4 v90, s[88:91], s85 offen lds\n"
        "s_add_u32 s85, s86, s71\n"
        "s_add_u32 m0, s87, 22528\n"
        "s_nop 0\n"
        "buffer_load_dwordx4 v90, s[88:91], s85 offen lds\n"
        "s_add_u32 s85, s86, s72\n"
        "s_add_u32 m0, s87, 24576\n"
        "s_nop 0\n"
        "buffer_load_dwordx4 v90, s[88:91], s85 offen lds\n"
        "s_add_u32 s85, s86, s73\n"
        "s_add_u32 m0, s87, 26624\n"
        "s_nop 0\n"
        "buffer_load_dwordx4 v90, s[88:91], s85 offen lds\n"
        "s_add_u32 s85, s86, s74\n"
        "s_add_u32 m0, s87, 28672\n"
        "s_nop 0\n"
        "buffer_load_dwordx4 v90, s[88:91], s85 offen lds\n"
        "s_add_u32 s85, s86, s75\n"
        "s_add_u32 m0, s87, 30720\n"
        "s_nop 0\n"
        "buffer_load_dwordx4 v90, s[88:91], s85 offen lds\n"
        "s_add_u32 s85, s86, s76\n"
        "s_add_u32 m0, s87, 32768\n"
        "s_nop 0\n"
        "buffer_load_dwordx4 v90, s[88:91], s85 offen lds\n"
        "s_add_u32 s85, s86, s77\n"
        "s_add_u32 m0, s87, 34816\n"
        "s_nop 0\n"
        "buffer_load_dwordx4 v90, s[88:91], s85 offen lds\n"
        "s_waitcnt vmcnt(18)\n"
        "s_barrier\n"
        "s_add_u32 s86, s39, 5\n"
        "s_sub_u32 s78, s86, 11\n"
        "s_min_u32 s86, s86, s78\n"
        "s_add_u32 s86, s86, s27\n"
        "s_add_u32 s86, s86, 8\n"
        "s_mul_i32 s86, s86, 18432\n"
        "s_add_u32 s85, s86, s69\n"
        "s_add_u32 m0, s87, 103680\n"
        "s_nop 0\n"
        "buffer_load_dwordx4 v90, s[88:91], s85 offen lds\n"
        "s_add_u32 s85, s86, s70\n"
        "s_add_u32 m0, s87, 105728\n"
        "s_nop 0\n"
        "buffer_load_dwordx4 v90, s[88:91], s85 offen lds\n"
        "s_add_u32 s85, s86, s71\n"
        "s_add_u32 m0, s87, 107776\n"
        "s_nop 0\n"
        "buffer_load_dwordx4 v90, s[88:91], s85 offen lds\n"
        "s_add_u32 s85, s86, s72\n"
        "s_add_u32 m0, s87, 109824\n"
        "s_nop 0\n"
        "buffer_load_dwordx4 v90, s[88:91], s85 offen lds\n"
        "s_add_u32 s85, s86, s73\n"
        "s_add_u32 m0, s87, 111872\n"
        "s_nop 0\n"
        "buffer_load_dwordx4 v90, s[88:91], s85 offen lds\n"
        "s_add_u32 s85, s86, s74\n"
        "s_add_u32 m0, s87, 113920\n"
        "s_nop 0\n"
        "buffer_load_dwordx4 v90, s[88:91], s85 offen lds\n"
        "s_add_u32 s85, s86, s75\n"
        "s_add_u32 m0, s87, 115968\n"
        "s_nop 0\n"
        "buffer_load_dwordx4 v90, s[88:91], s85 offen lds\n"
        "s_add_u32 s85, s86, s76\n"
        "s_add_u32 m0, s87, 118016\n"
        "s_nop 0\n"
        "buffer_load_dwordx4 v90, s[88:91], s85 offen lds\n"
        "s_add_u32 s85, s86, s77\n"
        "s_add_u32 m0, s87, 120064\n"
        "s_nop 0\n"
        "buffer_load_dwordx4 v90, s[88:91], s85 offen lds\n"
        "s_waitcnt vmcnt(18)\n"
        "s_barrier\n"
        "s_add_u32 s86, s39, 6\n"
        "s_sub_u32 s78, s86, 11\n"
        "s_min_u32 s86, s86, s78\n"
        "s_add_u32 s86, s86, s27\n"
        "s_add_u32 s86, s86, 8\n"
        "s_mul_i32 s86, s86, 18432\n"
        "s_add_u32 s85, s86, s69\n"
        "s_add_u32 m0, s87, 122112\n"
        "s_nop 0\n"
        "buffer_load_dwordx4 v90, s[88:91], s85 offen lds\n"
        "s_add_u32 s85, s86, s70\n"
        "s_add_u32 m0, s87, 124160\n"
        "s_nop 0\n"
        "buffer_load_dwordx4 v90, s[88:91], s85 offen lds\n"
        "s_add_u32 s85, s86, s71\n"
        "s_add_u32 m0, s87, 126208\n"
        "s_nop 0\n"
        "buffer_load_dwordx4 v90, s[88:91], s85 offen lds\n"
        "s_add_u32 s85, s86, s72\n"
        "s_add_u32 m0, s87, 128256\n"
        "s_nop 0\n"
        "buffer_load_dwordx4 v90, s[88:91], s85 offen lds\n"
        "s_add_u32 s85, s86, s73\n"
        "s_add_u32 m0, s87, 130304\n"
        "s_nop 0\n"
        "buffer_load_dwordx4 v90, s[88:91], s85 offen lds\n"
        "s_add_u32 s85, s86, s74\n"
        "s_add_u32 m0, s87, 132352\n"
        "s_nop 0\n"
        "buffer_load_dwordx4 v90, s[88:91], s85 offen lds\n"
        "s_add_u32 s85, s86, s75\n"
        "s_add_u32 m0, s87, 134400\n"
        "s_nop 0\n"
        "buffer_load_dwordx4 v90, s[88:91], s85 offen lds\n"
        "s_add_u32 s85, s86, s76\n"
        "s_add_u32 m0, s87, 136448\n"
        "s_nop 0\n"
        "buffer_load_dwordx4 v90, s[88:91], s85 offen lds\n"
        "s_add_u32 s85, s86, s77\n"
        "s_add_u32 m0, s87, 138496\n"
        "s_nop 0\n"
        "buffer_load_dwordx4 v90, s[88:91], s85 offen lds\n"
        "s_waitcnt vmcnt(18)\n"
        "s_barrier\n"
        "s_add_u32 s86, s39, 7\n"
        "s_sub_u32 s78, s86, 11\n"
        "s_min_u32 s86, s86, s78\n"
        "s_add_u32 s86, s86, s27\n"
        "s_add_u32 s86, s86, 8\n"
        "s_mul_i32 s86, s86, 18432\n"
        "s_add_u32 s85, s86, s69\n"
        "s_add_u32 m0, s87, 140544\n"
        "s_nop 0\n"
        "buffer_load_dwordx4 v90, s[88:91], s85 offen lds\n"
        "s_add_u32 s85, s86, s70\n"
        "s_add_u32 m0, s87, 142592\n"
        "s_nop 0\n"
        "buffer_load_dwordx4 v90, s[88:91], s85 offen lds\n"
        "s_add_u32 s85, s86, s71\n"
        "s_add_u32 m0, s87, 144640\n"
        "s_nop 0\n"
        "buffer_load_dwordx4 v90, s[88:91], s85 offen lds\n"
        "s_add_u32 s85, s86, s72\n"
        "s_add_u32 m0, s87, 146688\n"
        "s_nop 0\n"
        "buffer_load_dwordx4 v90, s[88:91], s85 offen lds\n"
        "s_add_u32 s85, s86, s73\n"
        "s_add_u32 m0, s87, 148736\n"
        "s_nop 0\n"
        "buffer_load_dwordx4 v90, s[88:91], s85 offen lds\n"
        "s_add_u32 s85, s86, s74\n"
        "s_add_u32 m0, s87, 150784\n"
        "s_nop 0\n"
        "buffer_load_dwordx4 v90, s[88:91], s85 offen lds\n"
        "s_add_u32 s85, s86, s75\n"
        "s_add_u32 m0, s87, 152832\n"
        "s_nop 0\n"
        "buffer_load_dwordx4 v90, s[88:91], s85 offen lds\n"
        "s_add_u32 s85, s86, s76\n"
        "s_add_u32 m0, s87, 154880\n"
        "s_nop 0\n"
        "buffer_load_dwordx4 v90, s[88:91], s85 offen lds\n"
        "s_add_u32 s85, s86, s77\n"
        "s_add_u32 m0, s87, 156928\n"
        "s_nop 0\n"
        "buffer_load_dwordx4 v90, s[88:91], s85 offen lds\n"
        "s_waitcnt vmcnt(18)\n"
        "s_barrier\n"
        "s_add_u32 s86, s39, 8\n"
        "s_sub_u32 s78, s86, 11\n"
        "s_min_u32 s86, s86, s78\n"
        "s_add_u32 s86, s86, s27\n"
        "s_add_u32 s86, s86, 8\n"
        "s_mul_i32 s86, s86, 18432\n"
        "s_add_u32 s85, s86, s69\n"
        "s_add_u32 m0, s87, 0\n"
        "s_nop 0\n"
        "buffer_load_dwordx4 v90, s[88:91], s85 offen lds\n"
        "s_add_u32 s85, s86, s70\n"
        "s_add_u32 m0, s87, 2048\n"
        "s_nop 0\n"
        "buffer_load_dwordx4 v90, s[88:91], s85 offen lds\n"
        "s_add_u32 s85, s86, s71\n"
        "s_add_u32 m0, s87, 4096\n"
        "s_nop 0\n"
        "buffer_load_dwordx4 v90, s[88:91], s85 offen lds\n"
        "s_add_u32 s85, s86, s72\n"
        "s_add_u32 m0, s87, 6144\n"
        "s_nop 0\n"
        "buffer_load_dwordx4 v90, s[88:91], s85 offen lds\n"
        "s_add_u32 s85, s86, s73\n"
        "s_add_u32 m0, s87, 8192\n"
        "s_nop 0\n"
        "buffer_load_dwordx4 v90, s[88:91], s85 offen lds\n"
        "s_add_u32 s85, s86, s74\n"
        "s_add_u32 m0, s87, 10240\n"
        "s_nop 0\n"
        "buffer_load_dwordx4 v90, s[88:91], s85 offen lds\n"
        "s_add_u32 s85, s86, s75\n"
        "s_add_u32 m0, s87, 12288\n"
        "s_nop 0\n"
        "buffer_load_dwordx4 v90, s[88:91], s85 offen lds\n"
        "s_add_u32 s85, s86, s76\n"
        "s_add_u32 m0, s87, 14336\n"
        "s_nop 0\n"
        "buffer_load_dwordx4 v90, s[88:91], s85 offen lds\n"
        "s_add_u32 s85, s86, s77\n"
        "s_add_u32 m0, s87, 16384\n"
        "s_nop 0\n"
        "buffer_load_dwordx4 v90, s[88:91], s85 offen lds\n"
        "s_waitcnt vmcnt(18)\n"
        "s_barrier\n"
        "s_add_u32 s86, s39, 9\n"
        "s_sub_u32 s78, s86, 11\n"
        "s_min_u32 s86, s86, s78\n"
        "s_add_u32 s86, s86, s27\n"
        "s_add_u32 s86, s86, 8\n"
        "s_mul_i32 s86, s86, 18432\n"
        "s_add_u32 s85, s86, s69\n"
        "s_add_u32 m0, s87, 18432\n"
        "s_nop 0\n"
        "buffer_load_dwordx4 v90, s[88:91], s85 offen lds\n"
        "s_add_u32 s85, s86, s70\n"
        "s_add_u32 m0, s87, 20480\n"
        "s_nop 0\n"
        "buffer_load_dwordx4 v90, s[88:91], s85 offen lds\n"
        "s_add_u32 s85, s86, s71\n"
        "s_add_u32 m0, s87, 22528\n"
        "s_nop 0\n"
        "buffer_load_dwordx4 v90, s[88:91], s85 offen lds\n"
        "s_add_u32 s85, s86, s72\n"
        "s_add_u32 m0, s87, 24576\n"
        "s_nop 0\n"
        "buffer_load_dwordx4 v90, s[88:91], s85 offen lds\n"
        "s_add_u32 s85, s86, s73\n"
        "s_add_u32 m0, s87, 26624\n"
        "s_nop 0\n"
        "buffer_load_dwordx4 v90, s[88:91], s85 offen lds\n"
        "s_add_u32 s85, s86, s74\n"
        "s_add_u32 m0, s87, 28672\n"
        "s_nop 0\n"
        "buffer_load_dwordx4 v90, s[88:91], s85 offen lds\n"
        "s_add_u32 s85, s86, s75\n"
        "s_add_u32 m0, s87, 30720\n"
        "s_nop 0\n"
        "buffer_load_dwordx4 v90, s[88:91], s85 offen lds\n"
        "s_add_u32 s85, s86, s76\n"
        "s_add_u32 m0, s87, 32768\n"
        "s_nop 0\n"
        "buffer_load_dwordx4 v90, s[88:91], s85 offen lds\n"
        "s_add_u32 s85, s86, s77\n"
        "s_add_u32 m0, s87, 34816\n"
        "s_nop 0\n"
        "buffer_load_dwordx4 v90, s[88:91], s85 offen lds\n"
        "s_waitcnt vmcnt(18)\n"
        "s_barrier\n"
        "s_add_u32 s86, s39, 10\n"
        "s_sub_u32 s78, s86, 11\n"
        "s_min_u32 s86, s86, s78\n"
        "s_add_u32 s86, s86, s27\n"
        "s_add_u32 s86, s86, 8\n"
        "s_mul_i32 s86, s86, 18432\n"
        "s_add_u32 s85, s86, s69\n"
        "s_add_u32 m0, s87, 103680\n"
        "s_nop 0\n"
        "buffer_load_dwordx4 v90, s[88:91], s85 offen lds\n"
        "s_add_u32 s85, s86, s70\n"
        "s_add_u32 m0, s87, 105728\n"
        "s_nop 0\n"
        "buffer_load_dwordx4 v90, s[88:91], s85 offen lds\n"
        "s_add_u32 s85, s86, s71\n"
        "s_add_u32 m0, s87, 107776\n"
        "s_nop 0\n"
        "buffer_load_dwordx4 v90, s[88:91], s85 offen lds\n"
        "s_add_u32 s85, s86, s72\n"
        "s_add_u32 m0, s87, 109824\n"
        "s_nop 0\n"
        "buffer_load_dwordx4 v90, s[88:91], s85 offen lds\n"
        "s_add_u32 s85, s86, s73\n"
        "s_add_u32 m0, s87, 111872\n"
        "s_nop 0\n"
        "buffer_load_dwordx4 v90, s[88:91], s85 offen lds\n"
        "s_add_u32 s85, s86, s74\n"
        "s_add_u32 m0, s87, 113920\n"
        "s_nop 0\n"
        "buffer_load_dwordx4 v90, s[88:91], s85 offen lds\n"
        "s_add_u32 s85, s86, s75\n"
        "s_add_u32 m0, s87, 115968\n"
        "s_nop 0\n"
        "buffer_load_dwordx4 v90, s[88:91], s85 offen lds\n"
        "s_add_u32 s85, s86, s76\n"
        "s_add_u32 m0, s87, 118016\n"
        "s_nop 0\n"
        "buffer_load_dwordx4 v90, s[88:91], s85 offen lds\n"
        "s_add_u32 s85, s86, s77\n"
        "s_add_u32 m0, s87, 120064\n"
        "s_nop 0\n"
        "buffer_load_dwordx4 v90, s[88:91], s85 offen lds\n"
        "s_waitcnt vmcnt(18)\n"
        "s_barrier\n"
        "s_waitcnt vmcnt(9)\n"
        "s_barrier\n"
        "s_waitcnt vmcnt(0)\n"
        "s_barrier\n"
        "s_barrier\n"
        "s_branch L_end_%=\n"
        "L_ex_%=:\n"
        "s_and_b32 s78, s82, 1\n"
        "s_lshl_b32 s79, s78, 3\n"
        "v_lshrrev_b32 v12, 4, v2\n"
        "v_add_u32 v12, s79, v12\n"
        "v_and_b32 v13, 15, v2\n"
        "s_add_u32 s78, s83, 49152\n"
        "v_add_u32 v14, 0, v12\n"
        "v_xor_b32 v15, v13, v14\n"
        "v_lshlrev_b32 v15, 4, v15\n"
        "v_lshl_add_u32 v15, v14, 8, v15\n"
        "v_add_u32 v91, s78, v15\n"
        "v_add_u32 v14, 4, v12\n"
        "v_xor_b32 v15, v13, v14\n"
        "v_lshlrev_b32 v15, 4, v15\n"
        "v_lshl_add_u32 v15, v14, 8, v15\n"
        "v_add_u32 v98, s78, v15\n"
        "s_movk_i32 s79, 9216\n"
        "v_mul_lo_u32 v14, v12, s79\n"
        "v_lshl_add_u32 v96, v13, 4, v14\n"
        "v_add_u32 v97, 36864, v96\n"
        "s_lshl_b32 s78, s26, 8\n"
        "s_add_u32 s52, s22, s78\n"
        "s_addc_u32 s53, s23, 0\n"
        "s_and_b32 s53, s53, 0xffff\n"
        "s_sub_u32 s54, 18874368, s78\n"
        "s_mov_b32 s55, 0x20000\n"
        "s_waitcnt vmcnt(8)\n"
        "s_waitcnt lgkmcnt(3)\n"
        "v_cvt_f32_i32 v40, v160\n"
        "v_cvt_f32_i32 v41, v164\n"
        "v_cvt_f32_i32 v42, v168\n"
        "v_cvt_f32_i32 v43, v172\n"
        "v_cvt_f32_i32 v44, v176\n"
        "v_cvt_f32_i32 v45, v180\n"
        "v_cvt_f32_i32 v46, v184\n"
        "v_cvt_f32_i32 v47, v188\n"
        "v_fma_f32 v40, v40, v16, -v20\n"
        "v_fma_f32 v41, v41, v16, -v20\n"
        "v_fma_f32 v42, v42, v16, -v20\n"
        "v_fma_f32 v43, v43, v16, -v20\n"
        "v_fma_f32 v44, v44, v16, -v20\n"
        "v_fma_f32 v45, v45, v16, -v20\n"
        "v_fma_f32 v46, v46, v16, -v20\n"
        "v_fma_f32 v47, v47, v16, -v20\n"
        "v_mul_f32 v40, v40, v24\n"
        "v_mul_f32 v41, v41, v25\n"
        "v_mul_f32 v42, v42, v26\n"
        "v_mul_f32 v43, v43, v27\n"
        "v_mul_f32 v44, v44, v28\n"
        "v_mul_f32 v45, v45, v29\n"
        "v_mul_f32 v46, v46, v30\n"
        "v_mul_f32 v47, v47, v31\n"
        "v_cvt_pk_bf16_f32 v56, v40, v41\n"
        "v_cvt_pk_bf16_f32 v57, v42, v43\n"
        "v_cvt_pk_bf16_f32 v58, v44, v45\n"
        "v_cvt_pk_bf16_f32 v59, v46, v47\n"
        "ds_write_b128 v88, v[56:59] offset:17408\n"
        "ds_read_b128 v[32:35], v89 offset:2048\n"
        "ds_read_b128 v[36:39], v89 offset:2064\n"
        "v_cvt_f32_i32 v48, v161\n"
        "v_cvt_f32_i32 v49, v165\n"
        "v_cvt_f32_i32 v50, v169\n"
        "v_cvt_f32_i32 v51, v173\n"
        "v_cvt_f32_i32 v52, v177\n"
        "v_cvt_f32_i32 v53, v181\n"
        "v_cvt_f32_i32 v54, v185\n"
        "v_cvt_f32_i32 v55, v189\n"
        "v_fma_f32 v48, v48, v17, -v21\n"
        "v_fma_f32 v49, v49, v17, -v21\n"
        "v_fma_f32 v50, v50, v17, -v21\n"
        "v_fma_f32 v51, v51, v17, -v21\n"
        "v_fma_f32 v52, v52, v17, -v21\n"
        "v_fma_f32 v53, v53, v17, -v21\n"
        "v_fma_f32 v54, v54, v17, -v21\n"
        "v_fma_f32 v55, v55, v17, -v21\n"
        "v_mul_f32 v48, v48, v24\n"
        "v_mul_f32 v49, v49, v25\n"
        "v_mul_f32 v50, v50, v26\n"
        "v_mul_f32 v51, v51, v27\n"
        "v_mul_f32 v52, v52, v28\n"
        "v_mul_f32 v53, v53, v29\n"
        "v_mul_f32 v54, v54, v30\n"
        "v_mul_f32 v55, v55, v31\n"
        "v_cvt_pk_bf16_f32 v60, v48, v49\n"
        "v_cvt_pk_bf16_f32 v61, v50, v51\n"
        "v_cvt_pk_bf16_f32 v62, v52, v53\n"
        "v_cvt_pk_bf16_f32 v63, v54, v55\n"
        "ds_write_b128 v88, v[60:63] offset:17472\n"
        "v_cvt_f32_i32 v40, v162\n"
        "v_cvt_f32_i32 v41, v166\n"
        "v_cvt_f32_i32 v42, v170\n"
        "v_cvt_f32_i32 v43, v174\n"
        "v_cvt_f32_i32 v44, v178\n"
        "v_cvt_f32_i32 v45, v182\n"
        "v_cvt_f32_i32 v46, v186\n"
        "v_cvt_f32_i32 v47, v190\n"
        "v_fma_f32 v40, v40, v18, -v22\n"
        "v_fma_f32 v41, v41, v18, -v22\n"
        "v_fma_f32 v42, v42, v18, -v22\n"
        "v_fma_f32 v43, v43, v18, -v22\n"
        "v_fma_f32 v44, v44, v18, -v22\n"
        "v_fma_f32 v45, v45, v18, -v22\n"
        "v_fma_f32 v46, v46, v18, -v22\n"
        "v_fma_f32 v47, v47, v18, -v22\n"
        "v_mul_f32 v40, v40, v24\n"
        "v_mul_f32 v41, v41, v25\n"
        "v_mul_f32 v42, v42, v26\n"
        "v_mul_f32 v43, v43, v27\n"
        "v_mul_f32 v44, v44, v28\n"
        "v_mul_f32 v45, v45, v29\n"
        "v_mul_f32 v46, v46, v30\n"
        "v_mul_f32 v47, v47, v31\n"
        "v_cvt_pk_bf16_f32 v64, v40, v41\n"
        "v_cvt_pk_bf16_f32 v65, v42, v43\n"
        "v_cvt_pk_bf16_f32 v66, v44, v45\n"
        "v_cvt_pk_bf16_f32 v67, v46, v47\n"
        "ds_write_b128 v88, v[64:67] offset:17536\n"
        "v_cvt_f32_i32 v48, v163\n"
        "v_cvt_f32_i32 v49, v167\n"
        "v_cvt_f32_i32 v50, v171\n"
        "v_cvt_f32_i32 v51, v175\n"
        "v_cvt_f32_i32 v52, v179\n"
        "v_cvt_f32_i32 v53, v183\n"
        "v_cvt_f32_i32 v54, v187\n"
        "v_cvt_f32_i32 v55, v191\n"
        "v_fma_f32 v48, v48, v19, -v23\n"
        "v_fma_f32 v49, v49, v19, -v23\n"
        "v_fma_f32 v50, v50, v19, -v23\n"
        "v_fma_f32 v51, v51, v19, -v23\n"
        "v_fma_f32 v52, v52, v19, -v23\n"
        "v_fma_f32 v53, v53, v19, -v23\n"
        "v_fma_f32 v54, v54, v19, -v23\n"
        "v_fma_f32 v55, v55, v19, -v23\n"
        "v_mul_f32 v48, v48, v24\n"
        "v_mul_f32 v49, v49, v25\n"
        "v_mul_f32 v50, v50, v26\n"
        "v_mul_f32 v51, v51, v27\n"
        "v_mul_f32 v52, v52, v28\n"
        "v_mul_f32 v53, v53, v29\n"
        "v_mul_f32 v54, v54, v30\n"
        "v_mul_f32 v55, v55, v31\n"
        "v_cvt_pk_bf16_f32 v68, v48, v49\n"
        "v_cvt_pk_bf16_f32 v69, v50, v51\n"
        "v_cvt_pk_bf16_f32 v70, v52, v53\n"
        "v_cvt_pk_bf16_f32 v71, v54, v55\n"
        "ds_write_b128 v88, v[68:71] offset:17600\n"
        "s_waitcnt lgkmcnt(6)\n"
        "s_barrier\n"
        "s_waitcnt vmcnt(0)\n"
        "s_waitcnt lgkmcnt(3)\n"
        "v_cvt_f32_i32 v40, v192\n"
        "v_cvt_f32_i32 v41, v196\n"
        "v_cvt_f32_i32 v42, v200\n"
        "v_cvt_f32_i32 v43, v204\n"
        "v_cvt_f32_i32 v44, v208\n"
        "v_cvt_f32_i32 v45, v212\n"
        "v_cvt_f32_i32 v46, v216\n"
        "v_cvt_f32_i32 v47, v220\n"
        "v_fma_f32 v40, v40, v16, -v20\n"
        "v_fma_f32 v41, v41, v16, -v20\n"
        "v_fma_f32 v42, v42, v16, -v20\n"
        "v_fma_f32 v43, v43, v16, -v20\n"
        "v_fma_f32 v44, v44, v16, -v20\n"
        "v_fma_f32 v45, v45, v16, -v20\n"
        "v_fma_f32 v46, v46, v16, -v20\n"
        "v_fma_f32 v47, v47, v16, -v20\n"
        "v_mul_f32 v40, v40, v32\n"
        "v_mul_f32 v41, v41, v33\n"
        "v_mul_f32 v42, v42, v34\n"
        "v_mul_f32 v43, v43, v35\n"
        "v_mul_f32 v44, v44, v36\n"
        "v_mul_f32 v45, v45, v37\n"
        "v_mul_f32 v46, v46, v38\n"
        "v_mul_f32 v47, v47, v39\n"
        "v_cvt_pk_bf16_f32 v56, v40, v41\n"
        "v_cvt_pk_bf16_f32 v57, v42, v43\n"
        "v_cvt_pk_bf16_f32 v58, v44, v45\n"
        "v_cvt_pk_bf16_f32 v59, v46, v47\n"
        "ds_write_b128 v88, v[56:59] offset:34816\n"
        "v_cvt_f32_i32 v48, v193\n"
        "v_cvt_f32_i32 v49, v197\n"
        "v_cvt_f32_i32 v50, v201\n"
        "v_cvt_f32_i32 v51, v205\n"
        "v_cvt_f32_i32 v52, v209\n"
        "v_cvt_f32_i32 v53, v213\n"
        "v_cvt_f32_i32 v54, v217\n"
        "v_cvt_f32_i32 v55, v221\n"
        "v_fma_f32 v48, v48, v17, -v21\n"
        "v_fma_f32 v49, v49, v17, -v21\n"
        "v_fma_f32 v50, v50, v17, -v21\n"
        "v_fma_f32 v51, v51, v17, -v21\n"
        "v_fma_f32 v52, v52, v17, -v21\n"
        "v_fma_f32 v53, v53, v17, -v21\n"
        "v_fma_f32 v54, v54, v17, -v21\n"
        "v_fma_f32 v55, v55, v17, -v21\n"
        "v_mul_f32 v48, v48, v32\n"
        "v_mul_f32 v49, v49, v33\n"
        "v_mul_f32 v50, v50, v34\n"
        "v_mul_f32 v51, v51, v35\n"
        "v_mul_f32 v52, v52, v36\n"
        "v_mul_f32 v53, v53, v37\n"
        "v_mul_f32 v54, v54, v38\n"
        "v_mul_f32 v55, v55, v39\n"
        "v_cvt_pk_bf16_f32 v60, v48, v49\n"
        "v_cvt_pk_bf16_f32 v61, v50, v51\n"
        "v_cvt_pk_bf16_f32 v62, v52, v53\n"
        "v_cvt_pk_bf16_f32 v63, v54, v55\n"
        "ds_write_b128 v88, v[60:63] offset:34880\n"
        "v_cvt_f32_i32 v40, v194\n"
        "v_cvt_f32_i32 v41, v198\n"
        "v_cvt_f32_i32 v42, v202\n"
        "v_cvt_f32_i32 v43, v206\n"
        "v_cvt_f32_i32 v44, v210\n"
        "v_cvt_f32_i32 v45, v214\n"
        "v_cvt_f32_i32 v46, v218\n"
        "v_cvt_f32_i32 v47, v222\n"
        "v_fma_f32 v40, v40, v18, -v22\n"
        "v_fma_f32 v41, v41, v18, -v22\n"
        "v_fma_f32 v42, v42, v18, -v22\n"
        "v_fma_f32 v43, v43, v18, -v22\n"
        "v_fma_f32 v44, v44, v18, -v22\n"
        "v_fma_f32 v45, v45, v18, -v22\n"
        "v_fma_f32 v46, v46, v18, -v22\n"
        "v_fma_f32 v47, v47, v18, -v22\n"
        "v_mul_f32 v40, v40, v32\n"
        "v_mul_f32 v41, v41, v33\n"
        "v_mul_f32 v42, v42, v34\n"
        "v_mul_f32 v43, v43, v35\n"
        "v_mul_f32 v44, v44, v36\n"
        "v_mul_f32 v45, v45, v37\n"
        "v_mul_f32 v46, v46, v38\n"
        "v_mul_f32 v47, v47, v39\n"
        "v_cvt_pk_bf16_f32 v64, v40, v41\n"
        "v_cvt_pk_bf16_f32 v65, v42, v43\n"
        "v_cvt_pk_bf16_f32 v66, v44, v45\n"
        "v_cvt_pk_bf16_f32 v67, v46, v47\n"
        "ds_write_b128 v88, v[64:67] offset:34944\n"
        "v_cvt_f32_i32 v48, v195\n"
        "v_cvt_f32_i32 v49, v199\n"
        "v_cvt_f32_i32 v50, v203\n"
        "v_cvt_f32_i32 v51, v207\n"
        "v_cvt_f32_i32 v52, v211\n"
        "v_cvt_f32_i32 v53, v215\n"
        "v_cvt_f32_i32 v54, v219\n"
        "v_cvt_f32_i32 v55, v223\n"
        "v_fma_f32 v48, v48, v19, -v23\n"
        "v_fma_f32 v49, v49, v19, -v23\n"
        "v_fma_f32 v50, v50, v19, -v23\n"
        "v_fma_f32 v51, v51, v19, -v23\n"
        "v_fma_f32 v52, v52, v19, -v23\n"
        "v_fma_f32 v53, v53, v19, -v23\n"
        "v_fma_f32 v54, v54, v19, -v23\n"
        "v_fma_f32 v55, v55, v19, -v23\n"
        "v_mul_f32 v48, v48, v32\n"
        "v_mul_f32 v49, v49, v33\n"
        "v_mul_f32 v50, v50, v34\n"
        "v_mul_f32 v51, v51, v35\n"
        "v_mul_f32 v52, v52, v36\n"
        "v_mul_f32 v53, v53, v37\n"
        "v_mul_f32 v54, v54, v38\n"
        "v_mul_f32 v55, v55, v39\n"
        "v_cvt_pk_bf16_f32 v68, v48, v49\n"
        "v_cvt_pk_bf16_f32 v69, v50, v51\n"
        "v_cvt_pk_bf16_f32 v70, v52, v53\n"
        "v_cvt_pk_bf16_f32 v71, v54, v55\n"
        "ds_write_b128 v88, v[68:71] offset:35008\n"
        "s_waitcnt lgkmcnt(4)\n"
        "s_barrier\n"
        "s_waitcnt lgkmcnt(0)\n"
        "s_barrier\n"
        "s_barrier\n"
        "ds_read_b128 v[100:103], v91 offset:0\n"
        "ds_read_b128 v[104:107], v98 offset:0\n"
        "s_add_u32 s86, s27, 0\n"
        "s_sub_u32 s79, s86, s28\n"
        "s_mul_i32 s86, s86, 147456\n"
        "s_cmp_lt_u32 s79, s29\n"
        "s_cselect_b32 s85, s86, 0x80000000\n"
        "s_waitcnt lgkmcnt(1)\n"
        "buffer_store_dwordx4 v[100:103], v96, s[52:55], s85 offen sc1 nt\n"
        "s_waitcnt lgkmcnt(0)\n"
        "buffer_store_dwordx4 v[104:107], v97, s[52:55], s85 offen sc1 nt\n"
        "s_barrier\n"
        "ds_read_b128 v[108:111], v91 offset:8192\n"
        "ds_read_b128 v[112:115], v98 offset:8192\n"
        "ds_read_b128 v[116:119], v91 offset:12288\n"
        "ds_read_b128 v[120:123], v98 offset:12288\n"
        "s_add_u32 s86, s27, 1\n"
        "s_sub_u32 s79, s86, s28\n"
        "s_mul_i32 s86, s86, 147456\n"
        "s_cmp_lt_u32 s79, s29\n"
        "s_cselect_b32 s85, s86, 0x80000000\n"
        "s_waitcnt lgkmcnt(3)\n"
        "buffer_store_dwordx4 v[108:111], v96, s[52:55], s85 offen sc1 nt\n"
        "s_waitcnt lgkmcnt(2)\n"
        "buffer_store_dwordx4 v[112:115], v97, s[52:55], s85 offen sc1 nt\n"
        "s_add_u32 s86, s39, 0\n"
        "s_sub_u32 s78, s86, 11\n"
        "s_min_u32 s86, s86, s78\n"
        "s_add_u32 s86, s86, s27\n"
        "s_add_u32 s86, s86, 8\n"
        "s_sub_u32 s79, s86, s28\n"
        "s_mul_i32 s86, s86, 147456\n"
        "s_cmp_lt_u32 s79, s29\n"
        "s_cselect_b32 s85, s86, 0x80000000\n"
        "s_waitcnt lgkmcnt(1)\n"
        "buffer_store_dwordx4 v[116:119], v96, s[52:55], s85 offen sc1 nt\n"
        "s_waitcnt lgkmcnt(0)\n"
        "buffer_store_dwordx4 v[120:123], v97, s[52:55], s85 offen sc1 nt\n"
        "s_barrier\n"
        "ds_read_b128 v[124:127], v91 offset:16384\n"
        "ds_read_b128 v[128:131], v98 offset:16384\n"
        "ds_read_b128 v[100:103], v91 offset:20480\n"
        "ds_read_b128 v[104:107], v98 offset:20480\n"
        "s_add_u32 s86, s27, 2\n"
        "s_sub_u32 s79, s86, s28\n"
        "s_mul_i32 s86, s86, 147456\n"
        "s_cmp_lt_u32 s79, s29\n"
        "s_cselect_b32 s85, s86, 0x80000000\n"
        "s_waitcnt lgkmcnt(3)\n"
        "buffer_store_dwordx4 v[124:127], v96, s[52:55], s85 offen sc1 nt\n"
        "s_waitcnt lgkmcnt(2)\n"
        "buffer_store_dwordx4 v[128:131], v97, s[52:55], s85 offen sc1 nt\n"
        "s_add_u32 s86, s39, 1\n"
        "s_sub_u32 s78, s86, 11\n"
        "s_min_u32 s86, s86, s78\n"
        "s_add_u32 s86, s86, s27\n"
        "s_add_u32 s86, s86, 8\n"
        "s_sub_u32 s79, s86, s28\n"
        "s_mul_i32 s86, s86, 147456\n"
        "s_cmp_lt_u32 s79, s29\n"
        "s_cselect_b32 s85, s86, 0x80000000\n"
        "s_waitcnt lgkmcnt(1)\n"
        "buffer_store_dwordx4 v[100:103], v96, s[52:55], s85 offen sc1 nt\n"
        "s_waitcnt lgkmcnt(0)\n"
        "buffer_store_dwordx4 v[104:107], v97, s[52:55], s85 offen sc1 nt\n"
        "s_barrier\n"
        "ds_read_b128 v[108:111], v91 offset:24576\n"
        "ds_read_b128 v[112:115], v98 offset:24576\n"
        "ds_read_b128 v[116:119], v91 offset:28672\n"
        "ds_read_b128 v[120:123], v98 offset:28672\n"
        "s_add_u32 s86, s27, 3\n"
        "s_sub_u32 s79, s86, s28\n"
        "s_mul_i32 s86, s86, 147456\n"
        "s_cmp_lt_u32 s79, s29\n"
        "s_cselect_b32 s85, s86, 0x80000000\n"
        "s_waitcnt lgkmcnt(3)\n"
        "buffer_store_dwordx4 v[108:111], v96, s[52:55], s85 offen sc1 nt\n"
        "s_waitcnt lgkmcnt(2)\n"
        "buffer_store_dwordx4 v[112:115], v97, s[52:55], s85 offen sc1 nt\n"
        "s_add_u32 s86, s39, 2\n"
        "s_sub_u32 s78, s86, 11\n"
        "s_min_u32 s86, s86, s78\n"
        "s_add_u32 s86, s86, s27\n"
        "s_add_u32 s86, s86, 8\n"
        "s_sub_u32 s79, s86, s28\n"
        "s_mul_i32 s86, s86, 147456\n"
        "s_cmp_lt_u32 s79, s29\n"
        "s_cselect_b32 s85, s86, 0x80000000\n"
        "s_waitcnt lgkmcnt(1)\n"
        "buffer_store_dwordx4 v[116:119], v96, s[52:55], s85 offen sc1 nt\n"
        "s_waitcnt lgkmcnt(0)\n"
        "buffer_store_dwordx4 v[120:123], v97, s[52:55], s85 offen sc1 nt\n"
        "s_barrier\n"
        "ds_read_b128 v[124:127], v91 offset:32768\n"
        "ds_read_b128 v[128:131], v98 offset:32768\n"
        "ds_read_b128 v[100:103], v91 offset:36864\n"
        "ds_read_b128 v[104:107], v98 offset:36864\n"
        "s_add_u32 s86, s27, 4\n"
        "s_sub_u32 s79, s86, s28\n"
        "s_mul_i32 s86, s86, 147456\n"
        "s_cmp_lt_u32 s79, s29\n"
        "s_cselect_b32 s85, s86, 0x80000000\n"
        "s_waitcnt lgkmcnt(3)\n"
        "buffer_store_dwordx4 v[124:127], v96, s[52:55], s85 offen sc1 nt\n"
        "s_waitcnt lgkmcnt(2)\n"
        "buffer_store_dwordx4 v[128:131], v97, s[52:55], s85 offen sc1 nt\n"
        "s_add_u32 s86, s39, 3\n"
        "s_sub_u32 s78, s86, 11\n"
        "s_min_u32 s86, s86, s78\n"
        "s_add_u32 s86, s86, s27\n"
        "s_add_u32 s86, s86, 8\n"
        "s_sub_u32 s79, s86, s28\n"
        "s_mul_i32 s86, s86, 147456\n"
        "s_cmp_lt_u32 s79, s29\n"
        "s_cselect_b32 s85, s86, 0x80000000\n"
        "s_waitcnt lgkmcnt(1)\n"
        "buffer_store_dwordx4 v[100:103], v96, s[52:55], s85 offen sc1 nt\n"
        "s_waitcnt lgkmcnt(0)\n"
        "buffer_store_dwordx4 v[104:107], v97, s[52:55], s85 offen sc1 nt\n"
        "s_barrier\n"
        "ds_read_b128 v[108:111], v91 offset:40960\n"
        "ds_read_b128 v[112:115], v98 offset:40960\n"
        "ds_read_b128 v[116:119], v91 offset:45056\n"
        "ds_read_b128 v[120:123], v98 offset:45056\n"
        "s_add_u32 s86, s27, 5\n"
        "s_sub_u32 s79, s86, s28\n"
        "s_mul_i32 s86, s86, 147456\n"
        "s_cmp_lt_u32 s79, s29\n"
        "s_cselect_b32 s85, s86, 0x80000000\n"
        "s_waitcnt lgkmcnt(3)\n"
        "buffer_store_dwordx4 v[108:111], v96, s[52:55], s85 offen sc1 nt\n"
        "s_waitcnt lgkmcnt(2)\n"
        "buffer_store_dwordx4 v[112:115], v97, s[52:55], s85 offen sc1 nt\n"
        "s_add_u32 s86, s39, 4\n"
        "s_sub_u32 s78, s86, 11\n"
        "s_min_u32 s86, s86, s78\n"
        "s_add_u32 s86, s86, s27\n"
        "s_add_u32 s86, s86, 8\n"
        "s_sub_u32 s79, s86, s28\n"
        "s_mul_i32 s86, s86, 147456\n"
        "s_cmp_lt_u32 s79, s29\n"
        "s_cselect_b32 s85, s86, 0x80000000\n"
        "s_waitcnt lgkmcnt(1)\n"
        "buffer_store_dwordx4 v[116:119], v96, s[52:55], s85 offen sc1 nt\n"
        "s_waitcnt lgkmcnt(0)\n"
        "buffer_store_dwordx4 v[120:123], v97, s[52:55], s85 offen sc1 nt\n"
        "s_barrier\n"
        "ds_read_b128 v[124:127], v91 offset:0\n"
        "ds_read_b128 v[128:131], v98 offset:0\n"
        "ds_read_b128 v[100:103], v91 offset:4096\n"
        "ds_read_b128 v[104:107], v98 offset:4096\n"
        "s_add_u32 s86, s27, 6\n"
        "s_sub_u32 s79, s86, s28\n"
        "s_mul_i32 s86, s86, 147456\n"
        "s_cmp_lt_u32 s79, s29\n"
        "s_cselect_b32 s85, s86, 0x80000000\n"
        "s_waitcnt lgkmcnt(3)\n"
        "buffer_store_dwordx4 v[124:127], v96, s[52:55], s85 offen sc1 nt\n"
        "s_waitcnt lgkmcnt(2)\n"
        "buffer_store_dwordx4 v[128:131], v97, s[52:55], s85 offen sc1 nt\n"
        "s_add_u32 s86, s39, 5\n"
        "s_sub_u32 s78, s86, 11\n"
        "s_min_u32 s86, s86, s78\n"
        "s_add_u32 s86, s86, s27\n"
        "s_add_u32 s86, s86, 8\n"
        "s_sub_u32 s79, s86, s28\n"
        "s_mul_i32 s86, s86, 147456\n"
        "s_cmp_lt_u32 s79, s29\n"
        "s_cselect_b32 s85, s86, 0x80000000\n"
        "s_waitcnt lgkmcnt(1)\n"
        "buffer_store_dwordx4 v[100:103], v96, s[52:55], s85 offen sc1 nt\n"
        "s_waitcnt lgkmcnt(0)\n"
        "buffer_store_dwordx4 v[104:107], v97, s[52:55], s85 offen sc1 nt\n"
        "s_barrier\n"
        "ds_read_b128 v[108:111], v91 offset:8192\n"
        "ds_read_b128 v[112:115], v98 offset:8192\n"
        "ds_read_b128 v[116:119], v91 offset:12288\n"
        "ds_read_b128 v[120:123], v98 offset:12288\n"
        "s_add_u32 s86, s27, 7\n"
        "s_sub_u32 s79, s86, s28\n"
        "s_mul_i32 s86, s86, 147456\n"
        "s_cmp_lt_u32 s79, s29\n"
        "s_cselect_b32 s85, s86, 0x80000000\n"
        "s_waitcnt lgkmcnt(3)\n"
        "buffer_store_dwordx4 v[108:111], v96, s[52:55], s85 offen sc1 nt\n"
        "s_waitcnt lgkmcnt(2)\n"
        "buffer_store_dwordx4 v[112:115], v97, s[52:55], s85 offen sc1 nt\n"
        "s_add_u32 s86, s39, 6\n"
        "s_sub_u32 s78, s86, 11\n"
        "s_min_u32 s86, s86, s78\n"
        "s_add_u32 s86, s86, s27\n"
        "s_add_u32 s86, s86, 8\n"
        "s_sub_u32 s79, s86, s28\n"
        "s_mul_i32 s86, s86, 147456\n"
        "s_cmp_lt_u32 s79, s29\n"
        "s_cselect_b32 s85, s86, 0x80000000\n"
        "s_waitcnt lgkmcnt(1)\n"
        "buffer_store_dwordx4 v[116:119], v96, s[52:55], s85 offen sc1 nt\n"
        "s_waitcnt lgkmcnt(0)\n"
        "buffer_store_dwordx4 v[120:123], v97, s[52:55], s85 offen sc1 nt\n"
        "s_barrier\n"
        "ds_read_b128 v[124:127], v91 offset:16384\n"
        "ds_read_b128 v[128:131], v98 offset:16384\n"
        "s_add_u32 s86, s39, 7\n"
        "s_sub_u32 s78, s86, 11\n"
        "s_min_u32 s86, s86, s78\n"
        "s_add_u32 s86, s86, s27\n"
        "s_add_u32 s86, s86, 8\n"
        "s_sub_u32 s79, s86, s28\n"
        "s_mul_i32 s86, s86, 147456\n"
        "s_cmp_lt_u32 s79, s29\n"
        "s_cselect_b32 s85, s86, 0x80000000\n"
        "s_waitcnt lgkmcnt(1)\n"
        "buffer_store_dwordx4 v[124:127], v96, s[52:55], s85 offen sc1 nt\n"
        "s_waitcnt lgkmcnt(0)\n"
        "buffer_store_dwordx4 v[128:131], v97, s[52:55], s85 offen sc1 nt\n"
        "s_barrier\n"
        "s_barrier\n"
        "ds_read_b128 v[100:103], v91 offset:24576\n"
        "ds_read_b128 v[104:107], v98 offset:24576\n"
        "ds_read_b128 v[108:111], v91 offset:32768\n"
        "ds_read_b128 v[112:115], v98 offset:32768\n"
        "s_add_u32 s86, s39, 8\n"
        "s_sub_u32 s78, s86, 11\n"
        "s_min_u32 s86, s86, s78\n"
        "s_add_u32 s86, s86, s27\n"
        "s_add_u32 s86, s86, 8\n"
        "s_sub_u32 s79, s86, s28\n"
        "s_mul_i32 s86, s86, 147456\n"
        "s_cmp_lt_u32 s79, s29\n"
        "s_cselect_b32 s85, s86, 0x80000000\n"
        "s_waitcnt lgkmcnt(3)\n"
        "buffer_store_dwordx4 v[100:103], v96, s[52:55], s85 offen sc1 nt\n"
        "s_waitcnt lgkmcnt(2)\n"
        "buffer_store_dwordx4 v[104:107], v97, s[52:55], s85 offen sc1 nt\n"
        "s_add_u32 s86, s39, 9\n"
        "s_sub_u32 s78, s86, 11\n"
        "s_min_u32 s86, s86, s78\n"
        "s_add_u32 s86, s86, s27\n"
        "s_add_u32 s86, s86, 8\n"
        "s_sub_u32 s79, s86, s28\n"
        "s_mul_i32 s86, s86, 147456\n"
        "s_cmp_lt_u32 s79, s29\n"
        "s_cselect_b32 s85, s86, 0x80000000\n"
        "s_waitcnt lgkmcnt(1)\n"
        "buffer_store_dwordx4 v[108:111], v96, s[52:55], s85 offen sc1 nt\n"
        "s_waitcnt lgkmcnt(0)\n"
        "buffer_store_dwordx4 v[112:115], v97, s[52:55], s85 offen sc1 nt\n"
        "s_branch L_end_%=\n"
        "L_cons_%=:\n"
        "s_mul_i32 s79, s82, 32\n"
        "v_lshrrev_b32 v12, 3, v2\n"
        "v_and_b32 v13, 7, v2\n"
        "s_movk_i32 s80, 1152\n"
        "s_add_u32 s78, s79, 0\n"
        "v_add_u32 v14, s78, v12\n"
        "v_bfe_u32 v14, v14, 1, 3\n"
        "v_xor_b32 v14, v13, v14\n"
        "v_add_u32 v15, 0, v12\n"
        "v_mul_lo_u32 v15, v15, s80\n"
        "v_lshl_add_u32 v15, v14, 4, v15\n"
        "v_mov_b32 v32, v15\n"
        "s_add_u32 s78, s79, 8\n"
        "v_add_u32 v14, s78, v12\n"
        "v_bfe_u32 v14, v14, 1, 3\n"
        "v_xor_b32 v14, v13, v14\n"
        "v_add_u32 v15, 8, v12\n"
        "v_mul_lo_u32 v15, v15, s80\n"
        "v_lshl_add_u32 v15, v14, 4, v15\n"
        "v_subrev_u32 v33, 1024, v15\n"
        "s_add_u32 s78, s79, 16\n"
        "v_add_u32 v14, s78, v12\n"
        "v_bfe_u32 v14, v14, 1, 3\n"
        "v_xor_b32 v14, v13, v14\n"
        "v_add_u32 v15, 16, v12\n"
        "v_mul_lo_u32 v15, v15, s80\n"
        "v_lshl_add_u32 v15, v14, 4, v15\n"
        "v_subrev_u32 v34, 2048, v15\n"
        "s_add_u32 s78, s79, 24\n"
        "v_add_u32 v14, s78, v12\n"
        "v_bfe_u32 v14, v14, 1, 3\n"
        "v_xor_b32 v14, v13, v14\n"
        "v_add_u32 v15, 24, v12\n"
        "v_mul_lo_u32 v15, v15, s80\n"
        "v_lshl_add_u32 v15, v14, 4, v15\n"
        "v_subrev_u32 v35, 3072, v15\n"
        "s_mul_i32 s84, s82, 4096\n"
        "s_add_u32 s84, s84, s83\n"
        "s_bfe_u32 s81, s30, 262144\n"
        "s_lshl_b32 s69, s81, 7\n"
        "s_lshl_b32 s78, s27, 4\n"
        "s_add_u32 s78, s78, s79\n"
        "s_mul_i32 s86, s78, 1152\n"
        "s_lshl_b32 s78, s82, 6\n"
        "v_add_u32 v20, s78, v2\n"
        "v_min_u32 v20, 143, v20\n"
        "v_lshrrev_b32 v21, 4, v20\n"
        "v_lshlrev_b32 v22, 2, v21\n"
        "v_cmp_eq_u32 vcc, 8, v21\n"
        "v_lshrrev_b32 v21, v22, s30\n"
        "v_and_b32 v21, 15, v21\n"
        "v_mov_b32 v22, s31\n"
        "v_cndmask_b32 v21, v21, v22, vcc\n"
        "v_and_b32 v22, 15, v20\n"
        "v_lshlrev_b32 v22, 3, v22\n"
        "v_lshl_add_u32 v21, v21, 7, v22\n"
        "s_lshl_b32 s78, s82, 1\n"
        "v_lshrrev_b32 v12, 5, v2\n"
        "v_add_u32 v12, s78, v12\n"
        "v_and_b32 v13, 31, v2\n"
        "s_mov_b32 s79, 147456\n"
        "v_mul_lo_u32 v14, v12, s79\n"
        "v_lshl_add_u32 v208, v13, 4, v14\n"
        "s_lshl_b32 s78, s26, 8\n"
        "v_lshl_add_u32 v14, v13, 3, s78\n"
        "v_lshlrev_b32 v15, 4, v12\n"
        "v_add_u32 v209, 18432, v208\n"
        "v_add_u32 v210, 18432, v209\n"
        "v_add_u32 v211, 18432, v210\n"
        "v_add_u32 v212, 18432, v211\n"
        "v_add_u32 v213, 18432, v212\n"
        "v_add_u32 v214, 18432, v213\n"
        "v_add_u32 v215, 18432, v214\n"
        "v_lshrrev_b32 v16, 2, v12\n"
        "v_lshlrev_b32 v16, 2, v16\n"
        "v_lshrrev_b32 v17, 3, v13\n"
        "v_add_u32 v16, v16, v17\n"
        "v_lshlrev_b32 v16, 1, v16\n"
        "v_and_b32 v17, 1, v13\n"
        "v_add_u32 v16, v16, v17\n"
        "v_mul_u32_u24 v16, 1088, v16\n"
        "v_and_b32 v17, 3, v12\n"
        "v_lshlrev_b32 v17, 4, v17\n"
        "v_bfe_u32 v18, v13, 1, 2\n"
        "v_add_u32 v17, v17, v18\n"
        "v_lshl_add_u32 v16, v17, 4, v16\n"
        "s_add_u32 s78, s83, 49152\n"
        "v_add_u32 v216, s78, v16\n"
        "s_waitcnt lgkmcnt(0)\n"
        "s_add_u32 s48, s12, s86\n"
        "s_addc_u32 s49, s13, 0\n"
        "s_and_b32 s49, s49, 0xffff\n"
        "s_sub_u32 s50, 2359296, s86\n"
        "s_mov_b32 s51, 0x20000\n"
        "s_add_u32 m0, s84, 0\n"
        "s_nop 0\n"
        "buffer_load_dwordx4 v32, s[48:51], s69 offen lds\n"
        "buffer_load_dwordx4 v33, s[48:51], s69 offen offset:1024 lds\n"
        "buffer_load_dwordx4 v34, s[48:51], s69 offen offset:2048 lds\n"
        "buffer_load_dwordx4 v35, s[48:51], s69 offen offset:3072 lds\n"
        "s_mov_b32 s40, s20\n"
        "s_and_b32 s41, s21, 0xffff\n"
        "s_mov_b32 s42, 1152\n"
        "s_mov_b32 s43, 0x20000\n"
        "buffer_load_dwordx2 v[22:23], v21, s[40:43], 0 offen\n"
        "s_bfe_u32 s81, s30, 262148\n"
        "s_mul_i32 s61, s81, 1179648\n"
        "s_lshl_b32 s81, s81, 7\n"
        "s_lshl_b32 s78, s26, 9\n"
        "s_mov_b32 s56, s16\n"
        "s_and_b32 s57, s17, 0xffff\n"
        "s_mov_b32 s58, 9216\n"
        "s_mov_b32 s59, 0x20000\n"
        "buffer_load_dwordx2 v[176:177], v14, s[56:59], 0 offen\n"
        "s_mov_b32 s88, s18\n"
        "s_and_b32 s89, s19, 0xffff\n"
        "s_mov_b32 s90, 9216\n"
        "s_mov_b32 s91, 0x20000\n"
        "buffer_load_dwordx2 v[178:179], v14, s[88:91], 0 offen\n"
        "s_mov_b32 s40, s20\n"
        "s_and_b32 s41, s21, 0xffff\n"
        "s_mov_b32 s42, 1152\n"
        "s_mov_b32 s43, 0x20000\n"
        "buffer_load_dwordx4 v[180:183], v15, s[40:43], s81 offen\n"
        "s_add_u32 s44, s14, s78\n"
        "s_addc_u32 s45, s15, 0\n"
        "s_and_b32 s45, s45, 0xffff\n"
        "s_sub_u32 s46, 10616832, s78\n"
        "s_mov_b32 s47, 0x20000\n"
        "buffer_load_dwordx4 v[224:227], v208, s[44:47], s61 offen\n"
        "buffer_load_dwordx4 v[228:231], v209, s[44:47], s61 offen\n"
        "buffer_load_dwordx4 v[232:235], v210, s[44:47], s61 offen\n"
        "buffer_load_dwordx4 v[236:239], v211, s[44:47], s61 offen\n"
        "buffer_load_dwordx4 v[240:243], v212, s[44:47], s61 offen\n"
        "buffer_load_dwordx4 v[244:247], v213, s[44:47], s61 offen\n"
        "buffer_load_dwordx4 v[248:251], v214, s[44:47], s61 offen\n"
        "buffer_load_dwordx4 v[252:255], v215, s[44:47], s61 offen\n"
        "s_bfe_u32 s81, s30, 262144\n"
        "s_lshl_b32 s69, s81, 7\n"
        "s_bfe_u32 s81, s30, 262148\n"
        "s_lshl_b32 s70, s81, 7\n"
        "s_bfe_u32 s81, s30, 262152\n"
        "s_lshl_b32 s71, s81, 7\n"
        "s_bfe_u32 s81, s30, 262156\n"
        "s_lshl_b32 s72, s81, 7\n"
        "s_bfe_u32 s81, s30, 262160\n"
        "s_lshl_b32 s73, s81, 7\n"
        "s_bfe_u32 s81, s30, 262164\n"
        "s_lshl_b32 s74, s81, 7\n"
        "s_bfe_u32 s81, s30, 262168\n"
        "s_lshl_b32 s75, s81, 7\n"
        "s_bfe_u32 s81, s30, 262172\n"
        "s_lshl_b32 s76, s81, 7\n"
        "s_mov_b32 s81, s31\n"
        "s_lshl_b32 s77, s81, 7\n"
        "s_add_u32 m0, s84, 16384\n"
        "s_nop 0\n"
        "buffer_load_dwordx4 v32, s[48:51], s70 offen lds\n"
        "buffer_load_dwordx4 v33, s[48:51], s70 offen offset:1024 lds\n"
        "buffer_load_dwordx4 v34, s[48:51], s70 offen offset:2048 lds\n"
        "buffer_load_dwordx4 v35, s[48:51], s70 offen offset:3072 lds\n"
        "v_lshrrev_b32 v16, 4, v2\n"
        "v_and_b32 v17, 15, v2\n"
        "v_lshrrev_b32 v18, 1, v17\n"
        "v_xor_b32 v18, v16, v18\n"
        "v_lshlrev_b32 v18, 4, v18\n"
        "v_lshl_add_u32 v18, v17, 7, v18\n"
        "v_add_u32 v3, s83, v18\n"
        "v_xor_b32 v4, 64, v3\n"
        "v_and_b32 v18, 3, v17\n"
        "v_lshlrev_b32 v18, 2, v18\n"
        "v_lshrrev_b32 v19, 2, v17\n"
        "v_add_u32 v18, v18, v19\n"
        "v_lshl_add_u32 v18, v16, 4, v18\n"
        "s_mul_i32 s78, s82, 2176\n"
        "s_add_u32 s78, s78, s83\n"
        "s_add_u32 s78, s78, 49152\n"
        "v_lshl_add_u32 v5, v18, 4, s78\n"
        "s_lshl_b32 s78, s82, 2\n"
        "v_add_u32 v18, s78, v16\n"
        "v_xor_b32 v18, v18, v17\n"
        "v_lshlrev_b32 v18, 4, v18\n"
        "v_lshl_add_u32 v18, v17, 8, v18\n"
        "s_add_u32 s78, s83, 49152\n"
        "v_add_u32 v6, s78, v18\n"
        "s_add_u32 s78, s83, 101376\n"
        "v_lshl_add_u32 v20, v20, 4, s78\n"
        "s_waitcnt vmcnt(15)\n"
        "v_lshlrev_b32 v16, 16, v22\n"
        "v_and_b32 v17, 0xffff0000, v22\n"
        "v_lshlrev_b32 v18, 16, v23\n"
        "v_and_b32 v19, 0xffff0000, v23\n"
        "ds_write_b128 v20, v[16:19]\n"
        "s_waitcnt vmcnt(16)\n"
        "s_waitcnt lgkmcnt(0)\n"
        "s_barrier\n"
        "ds_read_b128 v[112:115], v5 offset:0\n"
        "ds_read_b128 v[116:119], v5 offset:1088\n"
        "ds_read_b128 v[12:15], v3 offset:0\n"
        "ds_read_b128 v[16:19], v3 offset:2048\n"
        "ds_read_b128 v[120:123], v5 offset:8704\n"
        "ds_read_b128 v[124:127], v5 offset:9792\n"
        "ds_read_b128 v[20:23], v3 offset:4096\n"
        "ds_read_b128 v[24:27], v3 offset:6144\n"
        "s_add_u32 m0, s84, 32768\n"
        "s_nop 0\n"
        "buffer_load_dwordx4 v32, s[48:51], s71 offen lds\n"
        "buffer_load_dwordx4 v33, s[48:51], s71 offen offset:1024 lds\n"
        "buffer_load_dwordx4 v34, s[48:51], s71 offen offset:2048 lds\n"
        "buffer_load_dwordx4 v35, s[48:51], s71 offen offset:3072 lds\n"
        "s_waitcnt lgkmcnt(5)\n"
        "v_mfma_f32_16x16x32_bf16 v[48:51], v[112:115], v[12:15], 0\n"
        "ds_read_b128 v[28:31], v3 offset:8192\n"
        "v_mfma_f32_16x16x32_bf16 v[52:55], v[116:119], v[12:15], 0\n"
        "s_waitcnt lgkmcnt(5)\n"
        "v_mfma_f32_16x16x32_bf16 v[56:59], v[112:115], v[16:19], 0\n"
        "ds_read_b128 v[12:15], v3 offset:10240\n"
        "v_mfma_f32_16x16x32_bf16 v[60:63], v[116:119], v[16:19], 0\n"
        "s_waitcnt lgkmcnt(3)\n"
        "v_mfma_f32_16x16x32_bf16 v[64:67], v[112:115], v[20:23], 0\n"
        "ds_read_b128 v[16:19], v3 offset:12288\n"
        "v_mfma_f32_16x16x32_bf16 v[68:71], v[116:119], v[20:23], 0\n"
        "s_waitcnt lgkmcnt(3)\n"
        "v_mfma_f32_16x16x32_bf16 v[72:75], v[112:115], v[24:27], 0\n"
        "ds_read_b128 v[20:23], v3 offset:14336\n"
        "v_mfma_f32_16x16x32_bf16 v[76:79], v[116:119], v[24:27], 0\n"
        "s_waitcnt lgkmcnt(3)\n"
        "v_mfma_f32_16x16x32_bf16 v[80:83], v[112:115], v[28:31], 0\n"
        "ds_read_b128 v[24:27], v4 offset:0\n"
        "v_mfma_f32_16x16x32_bf16 v[84:87], v[116:119], v[28:31], 0\n"
        "s_waitcnt lgkmcnt(3)\n"
        "v_mfma_f32_16x16x32_bf16 v[88:91], v[112:115], v[12:15], 0\n"
        "ds_read_b128 v[28:31], v4 offset:2048\n"
        "v_mfma_f32_16x16x32_bf16 v[92:95], v[116:119], v[12:15], 0\n"
        "s_waitcnt lgkmcnt(3)\n"
        "v_mfma_f32_16x16x32_bf16 v[96:99], v[112:115], v[16:19], 0\n"
        "ds_read_b128 v[12:15], v4 offset:4096\n"
        "v_mfma_f32_16x16x32_bf16 v[100:103], v[116:119], v[16:19], 0\n"
        "s_waitcnt lgkmcnt(3)\n"
        "v_mfma_f32_16x16x32_bf16 v[104:107], v[112:115], v[20:23], 0\n"
        "ds_read_b128 v[16:19], v4 offset:6144\n"
        "v_mfma_f32_16x16x32_bf16 v[108:111], v[116:119], v[20:23], 0\n"
        "s_waitcnt lgkmcnt(3)\n"
        "v_mfma_f32_16x16x32_bf16 v[48:51], v[120:123], v[24:27], v[48:51]\n"
        "ds_read_b128 v[20:23], v4 offset:8192\n"
        "v_mfma_f32_16x16x32_bf16 v[52:55], v[124:127], v[24:27], v[52:55]\n"
        "s_waitcnt lgkmcnt(3)\n"
        "v_mfma_f32_16x16x32_bf16 v[56:59], v[120:123], v[28:31], v[56:59]\n"
        "ds_read_b128 v[24:27], v4 offset:10240\n"
        "v_mfma_f32_16x16x32_bf16 v[60:63], v[124:127], v[28:31], v[60:63]\n"
        "s_waitcnt lgkmcnt(3)\n"
        "v_mfma_f32_16x16x32_bf16 v[64:67], v[120:123], v[12:15], v[64:67]\n"
        "ds_read_b128 v[28:31], v4 offset:12288\n"
        "v_mfma_f32_16x16x32_bf16 v[68:71], v[124:127], v[12:15], v[68:71]\n"
        "s_waitcnt lgkmcnt(3)\n"
        "v_mfma_f32_16x16x32_bf16 v[72:75], v[120:123], v[16:19], v[72:75]\n"
        "ds_read_b128 v[12:15], v4 offset:14336\n"
        "v_mfma_f32_16x16x32_bf16 v[76:79], v[124:127], v[16:19], v[76:79]\n"
        "s_waitcnt lgkmcnt(3)\n"
        "v_mfma_f32_16x16x32_bf16 v[80:83], v[120:123], v[20:23], v[80:83]\n"
        "v_mfma_f32_16x16x32_bf16 v[84:87], v[124:127], v[20:23], v[84:87]\n"
        "s_waitcnt lgkmcnt(2)\n"
        "v_mfma_f32_16x16x32_bf16 v[88:91], v[120:123], v[24:27], v[88:91]\n"
        "v_mfma_f32_16x16x32_bf16 v[92:95], v[124:127], v[24:27], v[92:95]\n"
        "s_waitcnt lgkmcnt(1)\n"
        "v_mfma_f32_16x16x32_bf16 v[96:99], v[120:123], v[28:31], v[96:99]\n"
        "v_mfma_f32_16x16x32_bf16 v[100:103], v[124:127], v[28:31], v[100:103]\n"
        "s_waitcnt lgkmcnt(0)\n"
        "v_mfma_f32_16x16x32_bf16 v[104:107], v[120:123], v[12:15], v[104:107]\n"
        "v_mfma_f32_16x16x32_bf16 v[108:111], v[124:127], v[12:15], v[108:111]\n"
        "s_waitcnt vmcnt(16)\n"
        "v_lshlrev_b32 v160, 16, v176\n"
        "v_and_b32 v161, 0xffff0000, v176\n"
        "v_lshlrev_b32 v162, 16, v177\n"
        "v_and_b32 v163, 0xffff0000, v177\n"
        "v_lshlrev_b32 v164, 16, v178\n"
        "v_and_b32 v165, 0xffff0000, v178\n"
        "v_lshlrev_b32 v166, 16, v179\n"
        "v_and_b32 v167, 0xffff0000, v179\n"
        "v_lshlrev_b32 v168, 16, v180\n"
        "v_and_b32 v169, 0xffff0000, v180\n"
        "v_lshlrev_b32 v170, 16, v181\n"
        "v_and_b32 v171, 0xffff0000, v181\n"
        "v_lshlrev_b32 v172, 16, v182\n"
        "v_and_b32 v173, 0xffff0000, v182\n"
        "v_lshlrev_b32 v174, 16, v183\n"
        "v_and_b32 v175, 0xffff0000, v183\n"
        "v_mul_f32 v164, v164, v160\n"
        "v_mul_f32 v165, v165, v161\n"
        "v_mul_f32 v166, v166, v162\n"
        "v_mul_f32 v167, v167, v163\n"
        "s_waitcnt vmcnt(8)\n"
        "v_cvt_f32_i32 v176, v224\n"
        "v_cvt_f32_i32 v177, v228\n"
        "v_cvt_f32_i32 v178, v232\n"
        "v_cvt_f32_i32 v179, v236\n"
        "v_cvt_f32_i32 v180, v240\n"
        "v_cvt_f32_i32 v181, v244\n"
        "v_cvt_f32_i32 v182, v248\n"
        "v_cvt_f32_i32 v183, v252\n"
        "v_fma_f32 v176, v176, v160, -v164\n"
        "v_fma_f32 v177, v177, v160, -v164\n"
        "v_fma_f32 v178, v178, v160, -v164\n"
        "v_fma_f32 v179, v179, v160, -v164\n"
        "v_fma_f32 v180, v180, v160, -v164\n"
        "v_fma_f32 v181, v181, v160, -v164\n"
        "v_fma_f32 v182, v182, v160, -v164\n"
        "v_fma_f32 v183, v183, v160, -v164\n"
        "v_mul_f32 v176, v176, v168\n"
        "v_mul_f32 v177, v177, v169\n"
        "v_mul_f32 v178, v178, v170\n"
        "v_mul_f32 v179, v179, v171\n"
        "v_mul_f32 v180, v180, v172\n"
        "v_mul_f32 v181, v181, v173\n"
        "v_mul_f32 v182, v182, v174\n"
        "v_mul_f32 v183, v183, v175\n"
        "v_cvt_pk_bf16_f32 v192, v176, v177\n"
        "v_cvt_pk_bf16_f32 v193, v178, v179\n"
        "v_cvt_pk_bf16_f32 v194, v180, v181\n"
        "v_cvt_pk_bf16_f32 v195, v182, v183\n"
        "ds_write_b128 v216, v[192:195] offset:17408\n"
        "v_cvt_f32_i32 v184, v225\n"
        "v_cvt_f32_i32 v185, v229\n"
        "v_cvt_f32_i32 v186, v233\n"
        "v_cvt_f32_i32 v187, v237\n"
        "v_cvt_f32_i32 v188, v241\n"
        "v_cvt_f32_i32 v189, v245\n"
        "v_cvt_f32_i32 v190, v249\n"
        "v_cvt_f32_i32 v191, v253\n"
        "v_fma_f32 v184, v184, v161, -v165\n"
        "v_fma_f32 v185, v185, v161, -v165\n"
        "v_fma_f32 v186, v186, v161, -v165\n"
        "v_fma_f32 v187, v187, v161, -v165\n"
        "v_fma_f32 v188, v188, v161, -v165\n"
        "v_fma_f32 v189, v189, v161, -v165\n"
        "v_fma_f32 v190, v190, v161, -v165\n"
        "v_fma_f32 v191, v191, v161, -v165\n"
        "v_mul_f32 v184, v184, v168\n"
        "v_mul_f32 v185, v185, v169\n"
        "v_mul_f32 v186, v186, v170\n"
        "v_mul_f32 v187, v187, v171\n"
        "v_mul_f32 v188, v188, v172\n"
        "v_mul_f32 v189, v189, v173\n"
        "v_mul_f32 v190, v190, v174\n"
        "v_mul_f32 v191, v191, v175\n"
        "v_cvt_pk_bf16_f32 v196, v184, v185\n"
        "v_cvt_pk_bf16_f32 v197, v186, v187\n"
        "v_cvt_pk_bf16_f32 v198, v188, v189\n"
        "v_cvt_pk_bf16_f32 v199, v190, v191\n"
        "ds_write_b128 v216, v[196:199] offset:17472\n"
        "v_cvt_f32_i32 v176, v226\n"
        "v_cvt_f32_i32 v177, v230\n"
        "v_cvt_f32_i32 v178, v234\n"
        "v_cvt_f32_i32 v179, v238\n"
        "v_cvt_f32_i32 v180, v242\n"
        "v_cvt_f32_i32 v181, v246\n"
        "v_cvt_f32_i32 v182, v250\n"
        "v_cvt_f32_i32 v183, v254\n"
        "v_fma_f32 v176, v176, v162, -v166\n"
        "v_fma_f32 v177, v177, v162, -v166\n"
        "v_fma_f32 v178, v178, v162, -v166\n"
        "v_fma_f32 v179, v179, v162, -v166\n"
        "v_fma_f32 v180, v180, v162, -v166\n"
        "v_fma_f32 v181, v181, v162, -v166\n"
        "v_fma_f32 v182, v182, v162, -v166\n"
        "v_fma_f32 v183, v183, v162, -v166\n"
        "v_mul_f32 v176, v176, v168\n"
        "v_mul_f32 v177, v177, v169\n"
        "v_mul_f32 v178, v178, v170\n"
        "v_mul_f32 v179, v179, v171\n"
        "v_mul_f32 v180, v180, v172\n"
        "v_mul_f32 v181, v181, v173\n"
        "v_mul_f32 v182, v182, v174\n"
        "v_mul_f32 v183, v183, v175\n"
        "v_cvt_pk_bf16_f32 v200, v176, v177\n"
        "v_cvt_pk_bf16_f32 v201, v178, v179\n"
        "v_cvt_pk_bf16_f32 v202, v180, v181\n"
        "v_cvt_pk_bf16_f32 v203, v182, v183\n"
        "ds_write_b128 v216, v[200:203] offset:17536\n"
        "v_cvt_f32_i32 v184, v227\n"
        "v_cvt_f32_i32 v185, v231\n"
        "v_cvt_f32_i32 v186, v235\n"
        "v_cvt_f32_i32 v187, v239\n"
        "v_cvt_f32_i32 v188, v243\n"
        "v_cvt_f32_i32 v189, v247\n"
        "v_cvt_f32_i32 v190, v251\n"
        "v_cvt_f32_i32 v191, v255\n"
        "v_fma_f32 v184, v184, v163, -v167\n"
        "v_fma_f32 v185, v185, v163, -v167\n"
        "v_fma_f32 v186, v186, v163, -v167\n"
        "v_fma_f32 v187, v187, v163, -v167\n"
        "v_fma_f32 v188, v188, v163, -v167\n"
        "v_fma_f32 v189, v189, v163, -v167\n"
        "v_fma_f32 v190, v190, v163, -v167\n"
        "v_fma_f32 v191, v191, v163, -v167\n"
        "v_mul_f32 v184, v184, v168\n"
        "v_mul_f32 v185, v185, v169\n"
        "v_mul_f32 v186, v186, v170\n"
        "v_mul_f32 v187, v187, v171\n"
        "v_mul_f32 v188, v188, v172\n"
        "v_mul_f32 v189, v189, v173\n"
        "v_mul_f32 v190, v190, v174\n"
        "v_mul_f32 v191, v191, v175\n"
        "v_cvt_pk_bf16_f32 v204, v184, v185\n"
        "v_cvt_pk_bf16_f32 v205, v186, v187\n"
        "v_cvt_pk_bf16_f32 v206, v188, v189\n"
        "v_cvt_pk_bf16_f32 v207, v190, v191\n"
        "ds_write_b128 v216, v[204:207] offset:17600\n"
        "s_waitcnt vmcnt(4)\n"
        "s_waitcnt lgkmcnt(0)\n"
        "s_barrier\n"
        "ds_read_b128 v[128:131], v5 offset:17408\n"
        "ds_read_b128 v[132:135], v5 offset:18496\n"
        "ds_read_b128 v[16:19], v3 offset:16384\n"
        "ds_read_b128 v[20:23], v3 offset:18432\n"
        "ds_read_b128 v[136:139], v5 offset:26112\n"
        "ds_read_b128 v[140:143], v5 offset:27200\n"
        "ds_read_b128 v[24:27], v3 offset:20480\n"
        "ds_read_b128 v[28:31], v3 offset:22528\n"
        "s_add_u32 m0, s84, 0\n"
        "s_nop 0\n"
        "buffer_load_dwordx4 v32, s[48:51], s72 offen lds\n"
        "buffer_load_dwordx4 v33, s[48:51], s72 offen offset:1024 lds\n"
        "buffer_load_dwordx4 v34, s[48:51], s72 offen offset:2048 lds\n"
        "buffer_load_dwordx4 v35, s[48:51], s72 offen offset:3072 lds\n"
        "s_waitcnt lgkmcnt(5)\n"
        "v_mfma_f32_16x16x32_bf16 v[48:51], v[128:131], v[16:19], v[48:51]\n"
        "ds_read_b128 v[12:15], v3 offset:24576\n"
        "v_mfma_f32_16x16x32_bf16 v[52:55], v[132:135], v[16:19], v[52:55]\n"
        "s_waitcnt lgkmcnt(5)\n"
        "v_mfma_f32_16x16x32_bf16 v[56:59], v[128:131], v[20:23], v[56:59]\n"
        "ds_read_b128 v[16:19], v3 offset:26624\n"
        "v_mfma_f32_16x16x32_bf16 v[60:63], v[132:135], v[20:23], v[60:63]\n"
        "s_waitcnt lgkmcnt(3)\n"
        "v_mfma_f32_16x16x32_bf16 v[64:67], v[128:131], v[24:27], v[64:67]\n"
        "ds_read_b128 v[20:23], v3 offset:28672\n"
        "v_mfma_f32_16x16x32_bf16 v[68:71], v[132:135], v[24:27], v[68:71]\n"
        "s_waitcnt lgkmcnt(3)\n"
        "v_mfma_f32_16x16x32_bf16 v[72:75], v[128:131], v[28:31], v[72:75]\n"
        "ds_read_b128 v[24:27], v3 offset:30720\n"
        "v_mfma_f32_16x16x32_bf16 v[76:79], v[132:135], v[28:31], v[76:79]\n"
        "s_waitcnt lgkmcnt(3)\n"
        "v_mfma_f32_16x16x32_bf16 v[80:83], v[128:131], v[12:15], v[80:83]\n"
        "ds_read_b128 v[28:31], v4 offset:16384\n"
        "v_mfma_f32_16x16x32_bf16 v[84:87], v[132:135], v[12:15], v[84:87]\n"
        "s_waitcnt lgkmcnt(3)\n"
        "v_mfma_f32_16x16x32_bf16 v[88:91], v[128:131], v[16:19], v[88:91]\n"
        "ds_read_b128 v[12:15], v4 offset:18432\n"
        "v_mfma_f32_16x16x32_bf16 v[92:95], v[132:135], v[16:19], v[92:95]\n"
        "s_waitcnt lgkmcnt(3)\n"
        "v_mfma_f32_16x16x32_bf16 v[96:99], v[128:131], v[20:23], v[96:99]\n"
        "ds_read_b128 v[16:19], v4 offset:20480\n"
        "v_mfma_f32_16x16x32_bf16 v[100:103], v[132:135], v[20:23], v[100:103]\n"
        "s_waitcnt lgkmcnt(3)\n"
        "v_mfma_f32_16x16x32_bf16 v[104:107], v[128:131], v[24:27], v[104:107]\n"
        "ds_read_b128 v[20:23], v4 offset:22528\n"
        "v_mfma_f32_16x16x32_bf16 v[108:111], v[132:135], v[24:27], v[108:111]\n"
        "s_waitcnt lgkmcnt(3)\n"
        "v_mfma_f32_16x16x32_bf16 v[48:51], v[136:139], v[28:31], v[48:51]\n"
        "ds_read_b128 v[24:27], v4 offset:24576\n"
        "v_mfma_f32_16x16x32_bf16 v[52:55], v[140:143], v[28:31], v[52:55]\n"
        "s_waitcnt lgkmcnt(3)\n"
        "v_mfma_f32_16x16x32_bf16 v[56:59], v[136:139], v[12:15], v[56:59]\n"
        "ds_read_b128 v[28:31], v4 offset:26624\n"
        "v_mfma_f32_16x16x32_bf16 v[60:63], v[140:143], v[12:15], v[60:63]\n"
        "s_waitcnt lgkmcnt(3)\n"
        "v_mfma_f32_16x16x32_bf16 v[64:67], v[136:139], v[16:19], v[64:67]\n"
        "ds_read_b128 v[12:15], v4 offset:28672\n"
        "v_mfma_f32_16x16x32_bf16 v[68:71], v[140:143], v[16:19], v[68:71]\n"
        "s_waitcnt lgkmcnt(3)\n"
        "v_mfma_f32_16x16x32_bf16 v[72:75], v[136:139], v[20:23], v[72:75]\n"
        "ds_read_b128 v[16:19], v4 offset:30720\n"
        "v_mfma_f32_16x16x32_bf16 v[76:79], v[140:143], v[20:23], v[76:79]\n"
        "s_waitcnt lgkmcnt(3)\n"
        "v_mfma_f32_16x16x32_bf16 v[80:83], v[136:139], v[24:27], v[80:83]\n"
        "v_mfma_f32_16x16x32_bf16 v[84:87], v[140:143], v[24:27], v[84:87]\n"
        "s_waitcnt lgkmcnt(2)\n"
        "v_mfma_f32_16x16x32_bf16 v[88:91], v[136:139], v[28:31], v[88:91]\n"
        "v_mfma_f32_16x16x32_bf16 v[92:95], v[140:143], v[28:31], v[92:95]\n"
        "s_waitcnt lgkmcnt(1)\n"
        "v_mfma_f32_16x16x32_bf16 v[96:99], v[136:139], v[12:15], v[96:99]\n"
        "v_mfma_f32_16x16x32_bf16 v[100:103], v[140:143], v[12:15], v[100:103]\n"
        "s_waitcnt lgkmcnt(0)\n"
        "v_mfma_f32_16x16x32_bf16 v[104:107], v[136:139], v[16:19], v[104:107]\n"
        "v_mfma_f32_16x16x32_bf16 v[108:111], v[140:143], v[16:19], v[108:111]\n"
        "s_waitcnt vmcnt(4)\n"
        "s_barrier\n"
        "ds_read_b128 v[144:147], v5 offset:34816\n"
        "ds_read_b128 v[148:151], v5 offset:35904\n"
        "ds_read_b128 v[20:23], v3 offset:32768\n"
        "ds_read_b128 v[24:27], v3 offset:34816\n"
        "ds_read_b128 v[152:155], v5 offset:43520\n"
        "ds_read_b128 v[156:159], v5 offset:44608\n"
        "ds_read_b128 v[28:31], v3 offset:36864\n"
        "ds_read_b128 v[12:15], v3 offset:38912\n"
        "s_add_u32 m0, s84, 16384\n"
        "s_nop 0\n"
        "buffer_load_dwordx4 v32, s[48:51], s73 offen lds\n"
        "buffer_load_dwordx4 v33, s[48:51], s73 offen offset:1024 lds\n"
        "buffer_load_dwordx4 v34, s[48:51], s73 offen offset:2048 lds\n"
        "buffer_load_dwordx4 v35, s[48:51], s73 offen offset:3072 lds\n"
        "s_waitcnt lgkmcnt(5)\n"
        "v_mfma_f32_16x16x32_bf16 v[48:51], v[144:147], v[20:23], v[48:51]\n"
        "ds_read_b128 v[16:19], v3 offset:40960\n"
        "v_mfma_f32_16x16x32_bf16 v[52:55], v[148:151], v[20:23], v[52:55]\n"
        "s_waitcnt lgkmcnt(5)\n"
        "v_mfma_f32_16x16x32_bf16 v[56:59], v[144:147], v[24:27], v[56:59]\n"
        "ds_read_b128 v[20:23], v3 offset:43008\n"
        "v_mfma_f32_16x16x32_bf16 v[60:63], v[148:151], v[24:27], v[60:63]\n"
        "s_waitcnt lgkmcnt(3)\n"
        "v_mfma_f32_16x16x32_bf16 v[64:67], v[144:147], v[28:31], v[64:67]\n"
        "ds_read_b128 v[24:27], v3 offset:45056\n"
        "v_mfma_f32_16x16x32_bf16 v[68:71], v[148:151], v[28:31], v[68:71]\n"
        "s_waitcnt lgkmcnt(3)\n"
        "v_mfma_f32_16x16x32_bf16 v[72:75], v[144:147], v[12:15], v[72:75]\n"
        "ds_read_b128 v[28:31], v3 offset:47104\n"
        "v_mfma_f32_16x16x32_bf16 v[76:79], v[148:151], v[12:15], v[76:79]\n"
        "s_waitcnt lgkmcnt(3)\n"
        "v_mfma_f32_16x16x32_bf16 v[80:83], v[144:147], v[16:19], v[80:83]\n"
        "ds_read_b128 v[12:15], v4 offset:32768\n"
        "v_mfma_f32_16x16x32_bf16 v[84:87], v[148:151], v[16:19], v[84:87]\n"
        "s_waitcnt lgkmcnt(3)\n"
        "v_mfma_f32_16x16x32_bf16 v[88:91], v[144:147], v[20:23], v[88:91]\n"
        "ds_read_b128 v[16:19], v4 offset:34816\n"
        "v_mfma_f32_16x16x32_bf16 v[92:95], v[148:151], v[20:23], v[92:95]\n"
        "s_waitcnt lgkmcnt(3)\n"
        "v_mfma_f32_16x16x32_bf16 v[96:99], v[144:147], v[24:27], v[96:99]\n"
        "ds_read_b128 v[20:23], v4 offset:36864\n"
        "v_mfma_f32_16x16x32_bf16 v[100:103], v[148:151], v[24:27], v[100:103]\n"
        "s_waitcnt lgkmcnt(3)\n"
        "v_mfma_f32_16x16x32_bf16 v[104:107], v[144:147], v[28:31], v[104:107]\n"
        "ds_read_b128 v[24:27], v4 offset:38912\n"
        "v_mfma_f32_16x16x32_bf16 v[108:111], v[148:151], v[28:31], v[108:111]\n"
        "s_waitcnt lgkmcnt(3)\n"
        "v_mfma_f32_16x16x32_bf16 v[48:51], v[152:155], v[12:15], v[48:51]\n"
        "ds_read_b128 v[28:31], v4 offset:40960\n"
        "v_mfma_f32_16x16x32_bf16 v[52:55], v[156:159], v[12:15], v[52:55]\n"
        "s_waitcnt lgkmcnt(3)\n"
        "v_mfma_f32_16x16x32_bf16 v[56:59], v[152:155], v[16:19], v[56:59]\n"
        "ds_read_b128 v[12:15], v4 offset:43008\n"
        "v_mfma_f32_16x16x32_bf16 v[60:63], v[156:159], v[16:19], v[60:63]\n"
        "s_waitcnt lgkmcnt(3)\n"
        "v_mfma_f32_16x16x32_bf16 v[64:67], v[152:155], v[20:23], v[64:67]\n"
        "ds_read_b128 v[16:19], v4 offset:45056\n"
        "v_mfma_f32_16x16x32_bf16 v[68:71], v[156:159], v[20:23], v[68:71]\n"
        "s_waitcnt lgkmcnt(3)\n"
        "v_mfma_f32_16x16x32_bf16 v[72:75], v[152:155], v[24:27], v[72:75]\n"
        "ds_read_b128 v[20:23], v4 offset:47104\n"
        "v_mfma_f32_16x16x32_bf16 v[76:79], v[156:159], v[24:27], v[76:79]\n"
        "s_waitcnt lgkmcnt(3)\n"
        "v_mfma_f32_16x16x32_bf16 v[80:83], v[152:155], v[28:31], v[80:83]\n"
        "v_mfma_f32_16x16x32_bf16 v[84:87], v[156:159], v[28:31], v[84:87]\n"
        "s_waitcnt lgkmcnt(2)\n"
        "v_mfma_f32_16x16x32_bf16 v[88:91], v[152:155], v[12:15], v[88:91]\n"
        "v_mfma_f32_16x16x32_bf16 v[92:95], v[156:159], v[12:15], v[92:95]\n"
        "s_waitcnt lgkmcnt(1)\n"
        "v_mfma_f32_16x16x32_bf16 v[96:99], v[152:155], v[16:19], v[96:99]\n"
        "v_mfma_f32_16x16x32_bf16 v[100:103], v[156:159], v[16:19], v[100:103]\n"
        "s_waitcnt lgkmcnt(0)\n"
        "v_mfma_f32_16x16x32_bf16 v[104:107], v[152:155], v[20:23], v[104:107]\n"
        "v_mfma_f32_16x16x32_bf16 v[108:111], v[156:159], v[20:23], v[108:111]\n"
        "s_waitcnt vmcnt(4)\n"
        "s_barrier\n"
        "ds_read_b128 v[160:163], v5 offset:0\n"
        "ds_read_b128 v[164:167], v5 offset:1088\n"
        "ds_read_b128 v[24:27], v3 offset:0\n"
        "ds_read_b128 v[28:31], v3 offset:2048\n"
        "ds_read_b128 v[168:171], v5 offset:8704\n"
        "ds_read_b128 v[172:175], v5 offset:9792\n"
        "ds_read_b128 v[12:15], v3 offset:4096\n"
        "ds_read_b128 v[16:19], v3 offset:6144\n"
        "s_add_u32 m0, s84, 32768\n"
        "s_nop 0\n"
        "buffer_load_dwordx4 v32, s[48:51], s74 offen lds\n"
        "buffer_load_dwordx4 v33, s[48:51], s74 offen offset:1024 lds\n"
        "buffer_load_dwordx4 v34, s[48:51], s74 offen offset:2048 lds\n"
        "buffer_load_dwordx4 v35, s[48:51], s74 offen offset:3072 lds\n"
        "s_waitcnt lgkmcnt(5)\n"
        "v_mfma_f32_16x16x32_bf16 v[48:51], v[160:163], v[24:27], v[48:51]\n"
        "ds_read_b128 v[20:23], v3 offset:8192\n"
        "v_mfma_f32_16x16x32_bf16 v[52:55], v[164:167], v[24:27], v[52:55]\n"
        "s_waitcnt lgkmcnt(5)\n"
        "v_mfma_f32_16x16x32_bf16 v[56:59], v[160:163], v[28:31], v[56:59]\n"
        "ds_read_b128 v[24:27], v3 offset:10240\n"
        "v_mfma_f32_16x16x32_bf16 v[60:63], v[164:167], v[28:31], v[60:63]\n"
        "s_waitcnt lgkmcnt(3)\n"
        "v_mfma_f32_16x16x32_bf16 v[64:67], v[160:163], v[12:15], v[64:67]\n"
        "ds_read_b128 v[28:31], v3 offset:12288\n"
        "v_mfma_f32_16x16x32_bf16 v[68:71], v[164:167], v[12:15], v[68:71]\n"
        "s_waitcnt lgkmcnt(3)\n"
        "v_mfma_f32_16x16x32_bf16 v[72:75], v[160:163], v[16:19], v[72:75]\n"
        "ds_read_b128 v[12:15], v3 offset:14336\n"
        "v_mfma_f32_16x16x32_bf16 v[76:79], v[164:167], v[16:19], v[76:79]\n"
        "s_waitcnt lgkmcnt(3)\n"
        "v_mfma_f32_16x16x32_bf16 v[80:83], v[160:163], v[20:23], v[80:83]\n"
        "ds_read_b128 v[16:19], v4 offset:0\n"
        "v_mfma_f32_16x16x32_bf16 v[84:87], v[164:167], v[20:23], v[84:87]\n"
        "s_waitcnt lgkmcnt(3)\n"
        "v_mfma_f32_16x16x32_bf16 v[88:91], v[160:163], v[24:27], v[88:91]\n"
        "ds_read_b128 v[20:23], v4 offset:2048\n"
        "v_mfma_f32_16x16x32_bf16 v[92:95], v[164:167], v[24:27], v[92:95]\n"
        "s_waitcnt lgkmcnt(3)\n"
        "v_mfma_f32_16x16x32_bf16 v[96:99], v[160:163], v[28:31], v[96:99]\n"
        "ds_read_b128 v[24:27], v4 offset:4096\n"
        "v_mfma_f32_16x16x32_bf16 v[100:103], v[164:167], v[28:31], v[100:103]\n"
        "s_waitcnt lgkmcnt(3)\n"
        "v_mfma_f32_16x16x32_bf16 v[104:107], v[160:163], v[12:15], v[104:107]\n"
        "ds_read_b128 v[28:31], v4 offset:6144\n"
        "v_mfma_f32_16x16x32_bf16 v[108:111], v[164:167], v[12:15], v[108:111]\n"
        "s_waitcnt lgkmcnt(3)\n"
        "v_mfma_f32_16x16x32_bf16 v[48:51], v[168:171], v[16:19], v[48:51]\n"
        "ds_read_b128 v[12:15], v4 offset:8192\n"
        "v_mfma_f32_16x16x32_bf16 v[52:55], v[172:175], v[16:19], v[52:55]\n"
        "s_waitcnt lgkmcnt(3)\n"
        "v_mfma_f32_16x16x32_bf16 v[56:59], v[168:171], v[20:23], v[56:59]\n"
        "ds_read_b128 v[16:19], v4 offset:10240\n"
        "v_mfma_f32_16x16x32_bf16 v[60:63], v[172:175], v[20:23], v[60:63]\n"
        "s_waitcnt lgkmcnt(3)\n"
        "v_mfma_f32_16x16x32_bf16 v[64:67], v[168:171], v[24:27], v[64:67]\n"
        "ds_read_b128 v[20:23], v4 offset:12288\n"
        "v_mfma_f32_16x16x32_bf16 v[68:71], v[172:175], v[24:27], v[68:71]\n"
        "s_waitcnt lgkmcnt(3)\n"
        "v_mfma_f32_16x16x32_bf16 v[72:75], v[168:171], v[28:31], v[72:75]\n"
        "ds_read_b128 v[24:27], v4 offset:14336\n"
        "v_mfma_f32_16x16x32_bf16 v[76:79], v[172:175], v[28:31], v[76:79]\n"
        "s_waitcnt lgkmcnt(3)\n"
        "v_mfma_f32_16x16x32_bf16 v[80:83], v[168:171], v[12:15], v[80:83]\n"
        "v_mfma_f32_16x16x32_bf16 v[84:87], v[172:175], v[12:15], v[84:87]\n"
        "s_waitcnt lgkmcnt(2)\n"
        "v_mfma_f32_16x16x32_bf16 v[88:91], v[168:171], v[16:19], v[88:91]\n"
        "v_mfma_f32_16x16x32_bf16 v[92:95], v[172:175], v[16:19], v[92:95]\n"
        "s_waitcnt lgkmcnt(1)\n"
        "v_mfma_f32_16x16x32_bf16 v[96:99], v[168:171], v[20:23], v[96:99]\n"
        "v_mfma_f32_16x16x32_bf16 v[100:103], v[172:175], v[20:23], v[100:103]\n"
        "s_waitcnt lgkmcnt(0)\n"
        "v_mfma_f32_16x16x32_bf16 v[104:107], v[168:171], v[24:27], v[104:107]\n"
        "v_mfma_f32_16x16x32_bf16 v[108:111], v[172:175], v[24:27], v[108:111]\n"
        "s_waitcnt vmcnt(4)\n"
        "s_barrier\n"
        "ds_read_b128 v[176:179], v5 offset:17408\n"
        "ds_read_b128 v[180:183], v5 offset:18496\n"
        "ds_read_b128 v[28:31], v3 offset:16384\n"
        "ds_read_b128 v[12:15], v3 offset:18432\n"
        "ds_read_b128 v[184:187], v5 offset:26112\n"
        "ds_read_b128 v[188:191], v5 offset:27200\n"
        "ds_read_b128 v[16:19], v3 offset:20480\n"
        "ds_read_b128 v[20:23], v3 offset:22528\n"
        "s_add_u32 m0, s84, 0\n"
        "s_nop 0\n"
        "buffer_load_dwordx4 v32, s[48:51], s75 offen lds\n"
        "buffer_load_dwordx4 v33, s[48:51], s75 offen offset:1024 lds\n"
        "buffer_load_dwordx4 v34, s[48:51], s75 offen offset:2048 lds\n"
        "buffer_load_dwordx4 v35, s[48:51], s75 offen offset:3072 lds\n"
        "s_waitcnt lgkmcnt(5)\n"
        "v_mfma_f32_16x16x32_bf16 v[48:51], v[176:179], v[28:31], v[48:51]\n"
        "ds_read_b128 v[24:27], v3 offset:24576\n"
        "v_mfma_f32_16x16x32_bf16 v[52:55], v[180:183], v[28:31], v[52:55]\n"
        "s_waitcnt lgkmcnt(5)\n"
        "v_mfma_f32_16x16x32_bf16 v[56:59], v[176:179], v[12:15], v[56:59]\n"
        "ds_read_b128 v[28:31], v3 offset:26624\n"
        "v_mfma_f32_16x16x32_bf16 v[60:63], v[180:183], v[12:15], v[60:63]\n"
        "s_waitcnt lgkmcnt(3)\n"
        "v_mfma_f32_16x16x32_bf16 v[64:67], v[176:179], v[16:19], v[64:67]\n"
        "ds_read_b128 v[12:15], v3 offset:28672\n"
        "v_mfma_f32_16x16x32_bf16 v[68:71], v[180:183], v[16:19], v[68:71]\n"
        "s_waitcnt lgkmcnt(3)\n"
        "v_mfma_f32_16x16x32_bf16 v[72:75], v[176:179], v[20:23], v[72:75]\n"
        "ds_read_b128 v[16:19], v3 offset:30720\n"
        "v_mfma_f32_16x16x32_bf16 v[76:79], v[180:183], v[20:23], v[76:79]\n"
        "s_waitcnt lgkmcnt(3)\n"
        "v_mfma_f32_16x16x32_bf16 v[80:83], v[176:179], v[24:27], v[80:83]\n"
        "ds_read_b128 v[20:23], v4 offset:16384\n"
        "v_mfma_f32_16x16x32_bf16 v[84:87], v[180:183], v[24:27], v[84:87]\n"
        "s_waitcnt lgkmcnt(3)\n"
        "v_mfma_f32_16x16x32_bf16 v[88:91], v[176:179], v[28:31], v[88:91]\n"
        "ds_read_b128 v[24:27], v4 offset:18432\n"
        "v_mfma_f32_16x16x32_bf16 v[92:95], v[180:183], v[28:31], v[92:95]\n"
        "s_waitcnt lgkmcnt(3)\n"
        "v_mfma_f32_16x16x32_bf16 v[96:99], v[176:179], v[12:15], v[96:99]\n"
        "ds_read_b128 v[28:31], v4 offset:20480\n"
        "v_mfma_f32_16x16x32_bf16 v[100:103], v[180:183], v[12:15], v[100:103]\n"
        "s_waitcnt lgkmcnt(3)\n"
        "v_mfma_f32_16x16x32_bf16 v[104:107], v[176:179], v[16:19], v[104:107]\n"
        "ds_read_b128 v[12:15], v4 offset:22528\n"
        "v_mfma_f32_16x16x32_bf16 v[108:111], v[180:183], v[16:19], v[108:111]\n"
        "s_waitcnt lgkmcnt(3)\n"
        "v_mfma_f32_16x16x32_bf16 v[48:51], v[184:187], v[20:23], v[48:51]\n"
        "ds_read_b128 v[16:19], v4 offset:24576\n"
        "v_mfma_f32_16x16x32_bf16 v[52:55], v[188:191], v[20:23], v[52:55]\n"
        "s_waitcnt lgkmcnt(3)\n"
        "v_mfma_f32_16x16x32_bf16 v[56:59], v[184:187], v[24:27], v[56:59]\n"
        "ds_read_b128 v[20:23], v4 offset:26624\n"
        "v_mfma_f32_16x16x32_bf16 v[60:63], v[188:191], v[24:27], v[60:63]\n"
        "s_waitcnt lgkmcnt(3)\n"
        "v_mfma_f32_16x16x32_bf16 v[64:67], v[184:187], v[28:31], v[64:67]\n"
        "ds_read_b128 v[24:27], v4 offset:28672\n"
        "v_mfma_f32_16x16x32_bf16 v[68:71], v[188:191], v[28:31], v[68:71]\n"
        "s_waitcnt lgkmcnt(3)\n"
        "v_mfma_f32_16x16x32_bf16 v[72:75], v[184:187], v[12:15], v[72:75]\n"
        "ds_read_b128 v[28:31], v4 offset:30720\n"
        "v_mfma_f32_16x16x32_bf16 v[76:79], v[188:191], v[12:15], v[76:79]\n"
        "s_waitcnt lgkmcnt(3)\n"
        "v_mfma_f32_16x16x32_bf16 v[80:83], v[184:187], v[16:19], v[80:83]\n"
        "v_mfma_f32_16x16x32_bf16 v[84:87], v[188:191], v[16:19], v[84:87]\n"
        "s_waitcnt lgkmcnt(2)\n"
        "v_mfma_f32_16x16x32_bf16 v[88:91], v[184:187], v[20:23], v[88:91]\n"
        "v_mfma_f32_16x16x32_bf16 v[92:95], v[188:191], v[20:23], v[92:95]\n"
        "s_waitcnt lgkmcnt(1)\n"
        "v_mfma_f32_16x16x32_bf16 v[96:99], v[184:187], v[24:27], v[96:99]\n"
        "v_mfma_f32_16x16x32_bf16 v[100:103], v[188:191], v[24:27], v[100:103]\n"
        "s_waitcnt lgkmcnt(0)\n"
        "v_mfma_f32_16x16x32_bf16 v[104:107], v[184:187], v[28:31], v[104:107]\n"
        "v_mfma_f32_16x16x32_bf16 v[108:111], v[188:191], v[28:31], v[108:111]\n"
        "s_waitcnt vmcnt(4)\n"
        "s_barrier\n"
        "ds_read_b128 v[192:195], v5 offset:34816\n"
        "ds_read_b128 v[196:199], v5 offset:35904\n"
        "ds_read_b128 v[12:15], v3 offset:32768\n"
        "ds_read_b128 v[16:19], v3 offset:34816\n"
        "ds_read_b128 v[200:203], v5 offset:43520\n"
        "ds_read_b128 v[204:207], v5 offset:44608\n"
        "ds_read_b128 v[20:23], v3 offset:36864\n"
        "ds_read_b128 v[24:27], v3 offset:38912\n"
        "s_add_u32 m0, s84, 16384\n"
        "s_nop 0\n"
        "buffer_load_dwordx4 v32, s[48:51], s76 offen lds\n"
        "buffer_load_dwordx4 v33, s[48:51], s76 offen offset:1024 lds\n"
        "buffer_load_dwordx4 v34, s[48:51], s76 offen offset:2048 lds\n"
        "buffer_load_dwordx4 v35, s[48:51], s76 offen offset:3072 lds\n"
        "s_waitcnt lgkmcnt(5)\n"
        "v_mfma_f32_16x16x32_bf16 v[48:51], v[192:195], v[12:15], v[48:51]\n"
        "ds_read_b128 v[28:31], v3 offset:40960\n"
        "v_mfma_f32_16x16x32_bf16 v[52:55], v[196:199], v[12:15], v[52:55]\n"
        "s_waitcnt lgkmcnt(5)\n"
        "v_mfma_f32_16x16x32_bf16 v[56:59], v[192:195], v[16:19], v[56:59]\n"
        "ds_read_b128 v[12:15], v3 offset:43008\n"
        "v_mfma_f32_16x16x32_bf16 v[60:63], v[196:199], v[16:19], v[60:63]\n"
        "s_waitcnt lgkmcnt(3)\n"
        "v_mfma_f32_16x16x32_bf16 v[64:67], v[192:195], v[20:23], v[64:67]\n"
        "ds_read_b128 v[16:19], v3 offset:45056\n"
        "v_mfma_f32_16x16x32_bf16 v[68:71], v[196:199], v[20:23], v[68:71]\n"
        "s_waitcnt lgkmcnt(3)\n"
        "v_mfma_f32_16x16x32_bf16 v[72:75], v[192:195], v[24:27], v[72:75]\n"
        "ds_read_b128 v[20:23], v3 offset:47104\n"
        "v_mfma_f32_16x16x32_bf16 v[76:79], v[196:199], v[24:27], v[76:79]\n"
        "s_waitcnt lgkmcnt(3)\n"
        "v_mfma_f32_16x16x32_bf16 v[80:83], v[192:195], v[28:31], v[80:83]\n"
        "ds_read_b128 v[24:27], v4 offset:32768\n"
        "v_mfma_f32_16x16x32_bf16 v[84:87], v[196:199], v[28:31], v[84:87]\n"
        "s_waitcnt lgkmcnt(3)\n"
        "v_mfma_f32_16x16x32_bf16 v[88:91], v[192:195], v[12:15], v[88:91]\n"
        "ds_read_b128 v[28:31], v4 offset:34816\n"
        "v_mfma_f32_16x16x32_bf16 v[92:95], v[196:199], v[12:15], v[92:95]\n"
        "s_waitcnt lgkmcnt(3)\n"
        "v_mfma_f32_16x16x32_bf16 v[96:99], v[192:195], v[16:19], v[96:99]\n"
        "ds_read_b128 v[12:15], v4 offset:36864\n"
        "v_mfma_f32_16x16x32_bf16 v[100:103], v[196:199], v[16:19], v[100:103]\n"
        "s_waitcnt lgkmcnt(3)\n"
        "v_mfma_f32_16x16x32_bf16 v[104:107], v[192:195], v[20:23], v[104:107]\n"
        "ds_read_b128 v[16:19], v4 offset:38912\n"
        "v_mfma_f32_16x16x32_bf16 v[108:111], v[196:199], v[20:23], v[108:111]\n"
        "s_waitcnt lgkmcnt(3)\n"
        "v_mfma_f32_16x16x32_bf16 v[48:51], v[200:203], v[24:27], v[48:51]\n"
        "ds_read_b128 v[20:23], v4 offset:40960\n"
        "v_mfma_f32_16x16x32_bf16 v[52:55], v[204:207], v[24:27], v[52:55]\n"
        "s_waitcnt lgkmcnt(3)\n"
        "v_mfma_f32_16x16x32_bf16 v[56:59], v[200:203], v[28:31], v[56:59]\n"
        "ds_read_b128 v[24:27], v4 offset:43008\n"
        "v_mfma_f32_16x16x32_bf16 v[60:63], v[204:207], v[28:31], v[60:63]\n"
        "s_waitcnt lgkmcnt(3)\n"
        "v_mfma_f32_16x16x32_bf16 v[64:67], v[200:203], v[12:15], v[64:67]\n"
        "ds_read_b128 v[28:31], v4 offset:45056\n"
        "v_mfma_f32_16x16x32_bf16 v[68:71], v[204:207], v[12:15], v[68:71]\n"
        "s_waitcnt lgkmcnt(3)\n"
        "v_mfma_f32_16x16x32_bf16 v[72:75], v[200:203], v[16:19], v[72:75]\n"
        "ds_read_b128 v[12:15], v4 offset:47104\n"
        "v_mfma_f32_16x16x32_bf16 v[76:79], v[204:207], v[16:19], v[76:79]\n"
        "s_waitcnt lgkmcnt(3)\n"
        "v_mfma_f32_16x16x32_bf16 v[80:83], v[200:203], v[20:23], v[80:83]\n"
        "v_mfma_f32_16x16x32_bf16 v[84:87], v[204:207], v[20:23], v[84:87]\n"
        "s_waitcnt lgkmcnt(2)\n"
        "v_mfma_f32_16x16x32_bf16 v[88:91], v[200:203], v[24:27], v[88:91]\n"
        "v_mfma_f32_16x16x32_bf16 v[92:95], v[204:207], v[24:27], v[92:95]\n"
        "s_waitcnt lgkmcnt(1)\n"
        "v_mfma_f32_16x16x32_bf16 v[96:99], v[200:203], v[28:31], v[96:99]\n"
        "v_mfma_f32_16x16x32_bf16 v[100:103], v[204:207], v[28:31], v[100:103]\n"
        "s_waitcnt lgkmcnt(0)\n"
        "v_mfma_f32_16x16x32_bf16 v[104:107], v[200:203], v[12:15], v[104:107]\n"
        "v_mfma_f32_16x16x32_bf16 v[108:111], v[204:207], v[12:15], v[108:111]\n"
        "v_and_b32 v0, 15, v2\n"
        "s_movk_i32 s79, 9216\n"
        "v_mul_lo_u32 v0, v0, s79\n"
        "v_lshrrev_b32 v1, 4, v2\n"
        "v_lshl_add_u32 v0, v1, 4, v0\n"
        "s_lshl_b32 s78, s82, 6\n"
        "v_add_u32 v7, s78, v0\n"
        "s_lshl_b32 s78, s26, 8\n"
        "s_add_u32 s52, s22, s78\n"
        "s_addc_u32 s53, s23, 0\n"
        "s_and_b32 s53, s53, 0xffff\n"
        "s_sub_u32 s54, 18874368, s78\n"
        "s_mov_b32 s55, 0x20000\n"
        "s_add_u32 s86, s39, 10\n"
        "s_sub_u32 s78, s86, 11\n"
        "s_min_u32 s86, s86, s78\n"
        "s_add_u32 s86, s86, s27\n"
        "s_add_u32 s86, s86, 8\n"
        "s_sub_u32 s79, s86, s28\n"
        "s_mul_i32 s86, s86, 147456\n"
        "s_cmp_lt_u32 s79, s29\n"
        "s_cselect_b32 s85, s86, 0x80000000\n"
        "s_waitcnt vmcnt(4)\n"
        "s_barrier\n"
        "ds_read_b128 v[208:211], v5 offset:0\n"
        "ds_read_b128 v[212:215], v5 offset:1088\n"
        "ds_read_b128 v[16:19], v3 offset:0\n"
        "ds_read_b128 v[20:23], v3 offset:2048\n"
        "ds_read_b128 v[216:219], v5 offset:8704\n"
        "ds_read_b128 v[220:223], v5 offset:9792\n"
        "ds_read_b128 v[24:27], v3 offset:4096\n"
        "ds_read_b128 v[28:31], v3 offset:6144\n"
        "s_add_u32 m0, s84, 32768\n"
        "s_nop 0\n"
        "buffer_load_dwordx4 v32, s[48:51], s77 offen lds\n"
        "buffer_load_dwordx4 v33, s[48:51], s77 offen offset:1024 lds\n"
        "buffer_load_dwordx4 v34, s[48:51], s77 offen offset:2048 lds\n"
        "buffer_load_dwordx4 v35, s[48:51], s77 offen offset:3072 lds\n"
        "s_waitcnt lgkmcnt(5)\n"
        "v_mfma_f32_16x16x32_bf16 v[48:51], v[208:211], v[16:19], v[48:51]\n"
        "ds_read_b128 v[12:15], v3 offset:8192\n"
        "v_mfma_f32_16x16x32_bf16 v[52:55], v[212:215], v[16:19], v[52:55]\n"
        "s_waitcnt lgkmcnt(5)\n"
        "v_mfma_f32_16x16x32_bf16 v[56:59], v[208:211], v[20:23], v[56:59]\n"
        "ds_read_b128 v[16:19], v3 offset:10240\n"
        "v_mfma_f32_16x16x32_bf16 v[60:63], v[212:215], v[20:23], v[60:63]\n"
        "s_waitcnt lgkmcnt(3)\n"
        "v_mfma_f32_16x16x32_bf16 v[64:67], v[208:211], v[24:27], v[64:67]\n"
        "ds_read_b128 v[20:23], v3 offset:12288\n"
        "v_mfma_f32_16x16x32_bf16 v[68:71], v[212:215], v[24:27], v[68:71]\n"
        "s_waitcnt lgkmcnt(3)\n"
        "v_mfma_f32_16x16x32_bf16 v[72:75], v[208:211], v[28:31], v[72:75]\n"
        "ds_read_b128 v[24:27], v3 offset:14336\n"
        "v_mfma_f32_16x16x32_bf16 v[76:79], v[212:215], v[28:31], v[76:79]\n"
        "s_waitcnt lgkmcnt(3)\n"
        "v_mfma_f32_16x16x32_bf16 v[80:83], v[208:211], v[12:15], v[80:83]\n"
        "ds_read_b128 v[28:31], v4 offset:0\n"
        "v_mfma_f32_16x16x32_bf16 v[84:87], v[212:215], v[12:15], v[84:87]\n"
        "s_waitcnt lgkmcnt(3)\n"
        "v_mfma_f32_16x16x32_bf16 v[88:91], v[208:211], v[16:19], v[88:91]\n"
        "ds_read_b128 v[12:15], v4 offset:2048\n"
        "v_mfma_f32_16x16x32_bf16 v[92:95], v[212:215], v[16:19], v[92:95]\n"
        "s_waitcnt lgkmcnt(3)\n"
        "v_mfma_f32_16x16x32_bf16 v[96:99], v[208:211], v[20:23], v[96:99]\n"
        "ds_read_b128 v[16:19], v4 offset:4096\n"
        "v_mfma_f32_16x16x32_bf16 v[100:103], v[212:215], v[20:23], v[100:103]\n"
        "s_waitcnt lgkmcnt(3)\n"
        "v_mfma_f32_16x16x32_bf16 v[104:107], v[208:211], v[24:27], v[104:107]\n"
        "ds_read_b128 v[20:23], v4 offset:6144\n"
        "v_mfma_f32_16x16x32_bf16 v[108:111], v[212:215], v[24:27], v[108:111]\n"
        "s_waitcnt lgkmcnt(3)\n"
        "v_mfma_f32_16x16x32_bf16 v[48:51], v[216:219], v[28:31], v[48:51]\n"
        "ds_read_b128 v[24:27], v4 offset:8192\n"
        "v_mfma_f32_16x16x32_bf16 v[52:55], v[220:223], v[28:31], v[52:55]\n"
        "s_waitcnt lgkmcnt(3)\n"
        "v_mfma_f32_16x16x32_bf16 v[56:59], v[216:219], v[12:15], v[56:59]\n"
        "ds_read_b128 v[28:31], v4 offset:10240\n"
        "v_mfma_f32_16x16x32_bf16 v[60:63], v[220:223], v[12:15], v[60:63]\n"
        "s_waitcnt lgkmcnt(3)\n"
        "v_mfma_f32_16x16x32_bf16 v[64:67], v[216:219], v[16:19], v[64:67]\n"
        "ds_read_b128 v[12:15], v4 offset:12288\n"
        "v_mfma_f32_16x16x32_bf16 v[68:71], v[220:223], v[16:19], v[68:71]\n"
        "s_waitcnt lgkmcnt(3)\n"
        "v_mfma_f32_16x16x32_bf16 v[72:75], v[216:219], v[20:23], v[72:75]\n"
        "ds_read_b128 v[16:19], v4 offset:14336\n"
        "v_mfma_f32_16x16x32_bf16 v[76:79], v[220:223], v[20:23], v[76:79]\n"
        "s_waitcnt lgkmcnt(3)\n"
        "v_mfma_f32_16x16x32_bf16 v[80:83], v[216:219], v[24:27], v[80:83]\n"
        "v_mfma_f32_16x16x32_bf16 v[84:87], v[220:223], v[24:27], v[84:87]\n"
        "s_waitcnt lgkmcnt(2)\n"
        "v_mfma_f32_16x16x32_bf16 v[88:91], v[216:219], v[28:31], v[88:91]\n"
        "v_mfma_f32_16x16x32_bf16 v[92:95], v[220:223], v[28:31], v[92:95]\n"
        "s_waitcnt lgkmcnt(1)\n"
        "v_mfma_f32_16x16x32_bf16 v[96:99], v[216:219], v[12:15], v[96:99]\n"
        "v_mfma_f32_16x16x32_bf16 v[100:103], v[220:223], v[12:15], v[100:103]\n"
        "s_waitcnt lgkmcnt(0)\n"
        "v_mfma_f32_16x16x32_bf16 v[104:107], v[216:219], v[16:19], v[104:107]\n"
        "v_mfma_f32_16x16x32_bf16 v[108:111], v[220:223], v[16:19], v[108:111]\n"
        "s_waitcnt vmcnt(4)\n"
        "s_barrier\n"
        "ds_read_b128 v[224:227], v5 offset:17408\n"
        "ds_read_b128 v[228:231], v5 offset:18496\n"
        "ds_read_b128 v[20:23], v3 offset:16384\n"
        "ds_read_b128 v[24:27], v3 offset:18432\n"
        "ds_read_b128 v[232:235], v5 offset:26112\n"
        "ds_read_b128 v[236:239], v5 offset:27200\n"
        "ds_read_b128 v[28:31], v3 offset:20480\n"
        "ds_read_b128 v[12:15], v3 offset:22528\n"
        "s_waitcnt lgkmcnt(5)\n"
        "v_mfma_f32_16x16x32_bf16 v[48:51], v[224:227], v[20:23], v[48:51]\n"
        "ds_read_b128 v[16:19], v3 offset:24576\n"
        "v_mfma_f32_16x16x32_bf16 v[52:55], v[228:231], v[20:23], v[52:55]\n"
        "s_waitcnt lgkmcnt(5)\n"
        "v_mfma_f32_16x16x32_bf16 v[56:59], v[224:227], v[24:27], v[56:59]\n"
        "ds_read_b128 v[20:23], v3 offset:26624\n"
        "v_mfma_f32_16x16x32_bf16 v[60:63], v[228:231], v[24:27], v[60:63]\n"
        "s_waitcnt lgkmcnt(3)\n"
        "v_mfma_f32_16x16x32_bf16 v[64:67], v[224:227], v[28:31], v[64:67]\n"
        "ds_read_b128 v[24:27], v3 offset:28672\n"
        "v_mfma_f32_16x16x32_bf16 v[68:71], v[228:231], v[28:31], v[68:71]\n"
        "s_waitcnt lgkmcnt(3)\n"
        "v_mfma_f32_16x16x32_bf16 v[72:75], v[224:227], v[12:15], v[72:75]\n"
        "ds_read_b128 v[28:31], v3 offset:30720\n"
        "v_mfma_f32_16x16x32_bf16 v[76:79], v[228:231], v[12:15], v[76:79]\n"
        "s_waitcnt lgkmcnt(3)\n"
        "v_mfma_f32_16x16x32_bf16 v[80:83], v[224:227], v[16:19], v[80:83]\n"
        "ds_read_b128 v[12:15], v4 offset:16384\n"
        "v_mfma_f32_16x16x32_bf16 v[84:87], v[228:231], v[16:19], v[84:87]\n"
        "s_waitcnt lgkmcnt(3)\n"
        "v_mfma_f32_16x16x32_bf16 v[88:91], v[224:227], v[20:23], v[88:91]\n"
        "ds_read_b128 v[16:19], v4 offset:18432\n"
        "v_mfma_f32_16x16x32_bf16 v[92:95], v[228:231], v[20:23], v[92:95]\n"
        "s_waitcnt lgkmcnt(3)\n"
        "v_mfma_f32_16x16x32_bf16 v[96:99], v[224:227], v[24:27], v[96:99]\n"
        "ds_read_b128 v[20:23], v4 offset:20480\n"
        "v_mfma_f32_16x16x32_bf16 v[100:103], v[228:231], v[24:27], v[100:103]\n"
        "s_waitcnt lgkmcnt(3)\n"
        "v_mfma_f32_16x16x32_bf16 v[104:107], v[224:227], v[28:31], v[104:107]\n"
        "ds_read_b128 v[24:27], v4 offset:22528\n"
        "v_mfma_f32_16x16x32_bf16 v[108:111], v[228:231], v[28:31], v[108:111]\n"
        "s_waitcnt lgkmcnt(3)\n"
        "v_mfma_f32_16x16x32_bf16 v[48:51], v[232:235], v[12:15], v[48:51]\n"
        "ds_read_b128 v[28:31], v4 offset:24576\n"
        "v_mfma_f32_16x16x32_bf16 v[52:55], v[236:239], v[12:15], v[52:55]\n"
        "s_waitcnt lgkmcnt(3)\n"
        "v_mfma_f32_16x16x32_bf16 v[56:59], v[232:235], v[16:19], v[56:59]\n"
        "ds_read_b128 v[12:15], v4 offset:26624\n"
        "v_mfma_f32_16x16x32_bf16 v[60:63], v[236:239], v[16:19], v[60:63]\n"
        "s_waitcnt lgkmcnt(3)\n"
        "v_mfma_f32_16x16x32_bf16 v[64:67], v[232:235], v[20:23], v[64:67]\n"
        "ds_read_b128 v[16:19], v4 offset:28672\n"
        "v_mfma_f32_16x16x32_bf16 v[68:71], v[236:239], v[20:23], v[68:71]\n"
        "s_waitcnt lgkmcnt(3)\n"
        "v_mfma_f32_16x16x32_bf16 v[72:75], v[232:235], v[24:27], v[72:75]\n"
        "ds_read_b128 v[20:23], v4 offset:30720\n"
        "v_mfma_f32_16x16x32_bf16 v[76:79], v[236:239], v[24:27], v[76:79]\n"
        "s_waitcnt lgkmcnt(3)\n"
        "v_mfma_f32_16x16x32_bf16 v[80:83], v[232:235], v[28:31], v[80:83]\n"
        "v_mfma_f32_16x16x32_bf16 v[84:87], v[236:239], v[28:31], v[84:87]\n"
        "s_waitcnt lgkmcnt(2)\n"
        "v_mfma_f32_16x16x32_bf16 v[88:91], v[232:235], v[12:15], v[88:91]\n"
        "v_mfma_f32_16x16x32_bf16 v[92:95], v[236:239], v[12:15], v[92:95]\n"
        "s_waitcnt lgkmcnt(1)\n"
        "v_mfma_f32_16x16x32_bf16 v[96:99], v[232:235], v[16:19], v[96:99]\n"
        "v_mfma_f32_16x16x32_bf16 v[100:103], v[236:239], v[16:19], v[100:103]\n"
        "s_waitcnt lgkmcnt(0)\n"
        "v_mfma_f32_16x16x32_bf16 v[104:107], v[232:235], v[20:23], v[104:107]\n"
        "v_mfma_f32_16x16x32_bf16 v[108:111], v[236:239], v[20:23], v[108:111]\n"
        "s_waitcnt vmcnt(0)\n"
        "s_barrier\n"
        "ds_read_b128 v[240:243], v5 offset:34816\n"
        "ds_read_b128 v[244:247], v5 offset:35904\n"
        "ds_read_b128 v[24:27], v3 offset:32768\n"
        "ds_read_b128 v[28:31], v3 offset:34816\n"
        "ds_read_b128 v[248:251], v5 offset:43520\n"
        "ds_read_b128 v[252:255], v5 offset:44608\n"
        "ds_read_b128 v[12:15], v3 offset:36864\n"
        "ds_read_b128 v[16:19], v3 offset:38912\n"
        "s_waitcnt lgkmcnt(5)\n"
        "v_mfma_f32_16x16x32_bf16 v[48:51], v[240:243], v[24:27], v[48:51]\n"
        "ds_read_b128 v[20:23], v3 offset:40960\n"
        "v_mfma_f32_16x16x32_bf16 v[52:55], v[244:247], v[24:27], v[52:55]\n"
        "s_waitcnt lgkmcnt(5)\n"
        "v_mfma_f32_16x16x32_bf16 v[56:59], v[240:243], v[28:31], v[56:59]\n"
        "ds_read_b128 v[24:27], v3 offset:43008\n"
        "v_mfma_f32_16x16x32_bf16 v[60:63], v[244:247], v[28:31], v[60:63]\n"
        "s_waitcnt lgkmcnt(3)\n"
        "v_mfma_f32_16x16x32_bf16 v[64:67], v[240:243], v[12:15], v[64:67]\n"
        "ds_read_b128 v[28:31], v3 offset:45056\n"
        "v_mfma_f32_16x16x32_bf16 v[68:71], v[244:247], v[12:15], v[68:71]\n"
        "s_waitcnt lgkmcnt(3)\n"
        "v_mfma_f32_16x16x32_bf16 v[72:75], v[240:243], v[16:19], v[72:75]\n"
        "ds_read_b128 v[12:15], v3 offset:47104\n"
        "v_mfma_f32_16x16x32_bf16 v[76:79], v[244:247], v[16:19], v[76:79]\n"
        "s_waitcnt lgkmcnt(3)\n"
        "v_mfma_f32_16x16x32_bf16 v[80:83], v[240:243], v[20:23], v[80:83]\n"
        "ds_read_b128 v[16:19], v4 offset:32768\n"
        "v_mfma_f32_16x16x32_bf16 v[84:87], v[244:247], v[20:23], v[84:87]\n"
        "s_waitcnt lgkmcnt(3)\n"
        "v_mfma_f32_16x16x32_bf16 v[88:91], v[240:243], v[24:27], v[88:91]\n"
        "ds_read_b128 v[20:23], v4 offset:34816\n"
        "v_mfma_f32_16x16x32_bf16 v[92:95], v[244:247], v[24:27], v[92:95]\n"
        "s_waitcnt lgkmcnt(3)\n"
        "v_mfma_f32_16x16x32_bf16 v[96:99], v[240:243], v[28:31], v[96:99]\n"
        "ds_read_b128 v[24:27], v4 offset:36864\n"
        "v_mfma_f32_16x16x32_bf16 v[100:103], v[244:247], v[28:31], v[100:103]\n"
        "s_waitcnt lgkmcnt(3)\n"
        "v_mfma_f32_16x16x32_bf16 v[104:107], v[240:243], v[12:15], v[104:107]\n"
        "ds_read_b128 v[28:31], v4 offset:38912\n"
        "v_mfma_f32_16x16x32_bf16 v[108:111], v[244:247], v[12:15], v[108:111]\n"
        "s_waitcnt lgkmcnt(3)\n"
        "v_mfma_f32_16x16x32_bf16 v[48:51], v[248:251], v[16:19], v[48:51]\n"
        "ds_read_b128 v[12:15], v4 offset:40960\n"
        "v_mfma_f32_16x16x32_bf16 v[52:55], v[252:255], v[16:19], v[52:55]\n"
        "s_waitcnt lgkmcnt(3)\n"
        "v_mfma_f32_16x16x32_bf16 v[56:59], v[248:251], v[20:23], v[56:59]\n"
        "ds_read_b128 v[16:19], v4 offset:43008\n"
        "v_mfma_f32_16x16x32_bf16 v[60:63], v[252:255], v[20:23], v[60:63]\n"
        "s_waitcnt lgkmcnt(3)\n"
        "v_mfma_f32_16x16x32_bf16 v[64:67], v[248:251], v[24:27], v[64:67]\n"
        "ds_read_b128 v[20:23], v4 offset:45056\n"
        "v_mfma_f32_16x16x32_bf16 v[68:71], v[252:255], v[24:27], v[68:71]\n"
        "s_waitcnt lgkmcnt(3)\n"
        "v_mfma_f32_16x16x32_bf16 v[72:75], v[248:251], v[28:31], v[72:75]\n"
        "ds_read_b128 v[24:27], v4 offset:47104\n"
        "v_mfma_f32_16x16x32_bf16 v[76:79], v[252:255], v[28:31], v[76:79]\n"
        "s_waitcnt lgkmcnt(3)\n"
        "v_mfma_f32_16x16x32_bf16 v[80:83], v[248:251], v[12:15], v[80:83]\n"
        "v_add_u32 v3, 103680, v3\n"
        "v_add_u32 v4, 103680, v4\n"
        "ds_read_b128 v[28:31], v3 offset:0\n"
        "v_mfma_f32_16x16x32_bf16 v[84:87], v[252:255], v[12:15], v[84:87]\n"
        "s_waitcnt lgkmcnt(3)\n"
        "v_mfma_f32_16x16x32_bf16 v[88:91], v[248:251], v[16:19], v[88:91]\n"
        "ds_read_b128 v[12:15], v4 offset:0\n"
        "v_mfma_f32_16x16x32_bf16 v[92:95], v[252:255], v[16:19], v[92:95]\n"
        "s_waitcnt lgkmcnt(3)\n"
        "v_mfma_f32_16x16x32_bf16 v[96:99], v[248:251], v[20:23], v[96:99]\n"
        "ds_read_b128 v[16:19], v3 offset:2048\n"
        "v_mfma_f32_16x16x32_bf16 v[100:103], v[252:255], v[20:23], v[100:103]\n"
        "s_waitcnt lgkmcnt(3)\n"
        "v_mfma_f32_16x16x32_bf16 v[104:107], v[248:251], v[24:27], v[104:107]\n"
        "ds_read_b128 v[20:23], v4 offset:2048\n"
        "v_mfma_f32_16x16x32_bf16 v[108:111], v[252:255], v[24:27], v[108:111]\n"
        "s_waitcnt lgkmcnt(3)\n"
        "v_mfma_f32_16x16x32_bf16 v[32:35], v[112:115], v[28:31], 0\n"
        "ds_read_b128 v[24:27], v3 offset:4096\n"
        "v_mfma_f32_16x16x32_bf16 v[36:39], v[116:119], v[28:31], 0\n"
        "s_waitcnt lgkmcnt(3)\n"
        "v_mfma_f32_16x16x32_bf16 v[32:35], v[120:123], v[12:15], v[32:35]\n"
        "ds_read_b128 v[28:31], v4 offset:4096\n"
        "v_mfma_f32_16x16x32_bf16 v[36:39], v[124:127], v[12:15], v[36:39]\n"
        "s_waitcnt lgkmcnt(3)\n"
        "v_mfma_f32_16x16x32_bf16 v[32:35], v[128:131], v[16:19], v[32:35]\n"
        "ds_read_b128 v[12:15], v3 offset:6144\n"
        "v_mfma_f32_16x16x32_bf16 v[36:39], v[132:135], v[16:19], v[36:39]\n"
        "v_cvt_pk_bf16_f32 v8, v48, v49\n"
        "s_waitcnt lgkmcnt(3)\n"
        "v_mfma_f32_16x16x32_bf16 v[32:35], v[136:139], v[20:23], v[32:35]\n"
        "ds_read_b128 v[16:19], v4 offset:6144\n"
        "v_mfma_f32_16x16x32_bf16 v[36:39], v[140:143], v[20:23], v[36:39]\n"
        "v_cvt_pk_bf16_f32 v9, v50, v51\n"
        "s_waitcnt lgkmcnt(3)\n"
        "v_mfma_f32_16x16x32_bf16 v[32:35], v[144:147], v[24:27], v[32:35]\n"
        "ds_read_b128 v[20:23], v3 offset:8192\n"
        "v_mfma_f32_16x16x32_bf16 v[36:39], v[148:151], v[24:27], v[36:39]\n"
        "v_cvt_pk_bf16_f32 v10, v52, v53\n"
        "s_waitcnt lgkmcnt(3)\n"
        "v_mfma_f32_16x16x32_bf16 v[32:35], v[152:155], v[28:31], v[32:35]\n"
        "ds_read_b128 v[24:27], v4 offset:8192\n"
        "v_mfma_f32_16x16x32_bf16 v[36:39], v[156:159], v[28:31], v[36:39]\n"
        "v_cvt_pk_bf16_f32 v11, v54, v55\n"
        "s_waitcnt lgkmcnt(3)\n"
        "v_mfma_f32_16x16x32_bf16 v[32:35], v[160:163], v[12:15], v[32:35]\n"
        "ds_read_b128 v[28:31], v3 offset:10240\n"
        "v_mfma_f32_16x16x32_bf16 v[36:39], v[164:167], v[12:15], v[36:39]\n"
        "ds_write_b128 v6, v[8:11] offset:0\n"
        "s_waitcnt lgkmcnt(4)\n"
        "v_mfma_f32_16x16x32_bf16 v[32:35], v[168:171], v[16:19], v[32:35]\n"
        "ds_read_b128 v[12:15], v4 offset:10240\n"
        "v_mfma_f32_16x16x32_bf16 v[36:39], v[172:175], v[16:19], v[36:39]\n"
        "s_waitcnt lgkmcnt(4)\n"
        "v_mfma_f32_16x16x32_bf16 v[32:35], v[176:179], v[20:23], v[32:35]\n"
        "ds_read_b128 v[16:19], v3 offset:12288\n"
        "v_mfma_f32_16x16x32_bf16 v[36:39], v[180:183], v[20:23], v[36:39]\n"
        "s_waitcnt lgkmcnt(4)\n"
        "v_mfma_f32_16x16x32_bf16 v[32:35], v[184:187], v[24:27], v[32:35]\n"
        "ds_read_b128 v[20:23], v4 offset:12288\n"
        "v_mfma_f32_16x16x32_bf16 v[36:39], v[188:191], v[24:27], v[36:39]\n"
        "s_waitcnt lgkmcnt(4)\n"
        "v_mfma_f32_16x16x32_bf16 v[32:35], v[192:195], v[28:31], v[32:35]\n"
        "ds_read_b128 v[24:27], v3 offset:14336\n"
        "v_mfma_f32_16x16x32_bf16 v[36:39], v[196:199], v[28:31], v[36:39]\n"
        "s_waitcnt lgkmcnt(3)\n"
        "v_mfma_f32_16x16x32_bf16 v[32:35], v[200:203], v[12:15], v[32:35]\n"
        "ds_read_b128 v[28:31], v4 offset:14336\n"
        "v_mfma_f32_16x16x32_bf16 v[36:39], v[204:207], v[12:15], v[36:39]\n"
        "s_waitcnt lgkmcnt(3)\n"
        "v_mfma_f32_16x16x32_bf16 v[32:35], v[208:211], v[16:19], v[32:35]\n"
        "ds_read_b128 v[12:15], v3 offset:16384\n"
        "v_mfma_f32_16x16x32_bf16 v[36:39], v[212:215], v[16:19], v[36:39]\n"
        "s_waitcnt lgkmcnt(3)\n"
        "v_mfma_f32_16x16x32_bf16 v[32:35], v[216:219], v[20:23], v[32:35]\n"
        "ds_read_b128 v[16:19], v4 offset:16384\n"
        "v_mfma_f32_16x16x32_bf16 v[36:39], v[220:223], v[20:23], v[36:39]\n"
        "s_waitcnt lgkmcnt(7)\n"
        "s_barrier\n"
        "v_add_u32 v3, 18432, v3\n"
        "v_add_u32 v4, 18432, v4\n"
        "s_waitcnt lgkmcnt(3)\n"
        "v_mfma_f32_16x16x32_bf16 v[32:35], v[224:227], v[24:27], v[32:35]\n"
        "ds_read_b128 v[20:23], v3 offset:0\n"
        "v_mfma_f32_16x16x32_bf16 v[36:39], v[228:231], v[24:27], v[36:39]\n"
        "s_waitcnt lgkmcnt(3)\n"
        "v_mfma_f32_16x16x32_bf16 v[32:35], v[232:235], v[28:31], v[32:35]\n"
        "ds_read_b128 v[24:27], v4 offset:0\n"
        "v_mfma_f32_16x16x32_bf16 v[36:39], v[236:239], v[28:31], v[36:39]\n"
        "s_waitcnt lgkmcnt(3)\n"
        "v_mfma_f32_16x16x32_bf16 v[32:35], v[240:243], v[12:15], v[32:35]\n"
        "ds_read_b128 v[28:31], v3 offset:2048\n"
        "v_mfma_f32_16x16x32_bf16 v[36:39], v[244:247], v[12:15], v[36:39]\n"
        "s_waitcnt lgkmcnt(3)\n"
        "v_mfma_f32_16x16x32_bf16 v[32:35], v[248:251], v[16:19], v[32:35]\n"
        "ds_read_b128 v[12:15], v4 offset:2048\n"
        "v_mfma_f32_16x16x32_bf16 v[36:39], v[252:255], v[16:19], v[36:39]\n"
        "s_waitcnt lgkmcnt(3)\n"
        "v_mfma_f32_16x16x32_bf16 v[40:43], v[112:115], v[20:23], 0\n"
        "ds_read_b128 v[16:19], v3 offset:4096\n"
        "v_mfma_f32_16x16x32_bf16 v[44:47], v[116:119], v[20:23], 0\n"
        "s_waitcnt lgkmcnt(3)\n"
        "v_mfma_f32_16x16x32_bf16 v[40:43], v[120:123], v[24:27], v[40:43]\n"
        "ds_read_b128 v[20:23], v4 offset:4096\n"
        "v_mfma_f32_16x16x32_bf16 v[44:47], v[124:127], v[24:27], v[44:47]\n"
        "s_waitcnt lgkmcnt(3)\n"
        "v_mfma_f32_16x16x32_bf16 v[40:43], v[128:131], v[28:31], v[40:43]\n"
        "ds_read_b128 v[24:27], v3 offset:6144\n"
        "v_mfma_f32_16x16x32_bf16 v[44:47], v[132:135], v[28:31], v[44:47]\n"
        "v_cvt_pk_bf16_f32 v8, v56, v57\n"
        "s_waitcnt lgkmcnt(3)\n"
        "v_mfma_f32_16x16x32_bf16 v[40:43], v[136:139], v[12:15], v[40:43]\n"
        "ds_read_b128 v[28:31], v4 offset:6144\n"
        "v_mfma_f32_16x16x32_bf16 v[44:47], v[140:143], v[12:15], v[44:47]\n"
        "v_cvt_pk_bf16_f32 v9, v58, v59\n"
        "s_waitcnt lgkmcnt(3)\n"
        "v_mfma_f32_16x16x32_bf16 v[40:43], v[144:147], v[16:19], v[40:43]\n"
        "ds_read_b128 v[12:15], v3 offset:8192\n"
        "v_mfma_f32_16x16x32_bf16 v[44:47], v[148:151], v[16:19], v[44:47]\n"
        "v_cvt_pk_bf16_f32 v10, v60, v61\n"
        "s_waitcnt lgkmcnt(3)\n"
        "v_mfma_f32_16x16x32_bf16 v[40:43], v[152:155], v[20:23], v[40:43]\n"
        "ds_read_b128 v[16:19], v4 offset:8192\n"
        "v_mfma_f32_16x16x32_bf16 v[44:47], v[156:159], v[20:23], v[44:47]\n"
        "v_cvt_pk_bf16_f32 v11, v62, v63\n"
        "s_waitcnt lgkmcnt(3)\n"
        "v_mfma_f32_16x16x32_bf16 v[40:43], v[160:163], v[24:27], v[40:43]\n"
        "ds_read_b128 v[20:23], v3 offset:10240\n"
        "v_mfma_f32_16x16x32_bf16 v[44:47], v[164:167], v[24:27], v[44:47]\n"
        "ds_write_b128 v6, v[8:11] offset:8192\n"
        "s_waitcnt lgkmcnt(4)\n"
        "v_mfma_f32_16x16x32_bf16 v[40:43], v[168:171], v[28:31], v[40:43]\n"
        "ds_read_b128 v[24:27], v4 offset:10240\n"
        "v_mfma_f32_16x16x32_bf16 v[44:47], v[172:175], v[28:31], v[44:47]\n"
        "v_cvt_pk_bf16_f32 v8, v32, v33\n"
        "s_waitcnt lgkmcnt(4)\n"
        "v_mfma_f32_16x16x32_bf16 v[40:43], v[176:179], v[12:15], v[40:43]\n"
        "ds_read_b128 v[28:31], v3 offset:12288\n"
        "v_mfma_f32_16x16x32_bf16 v[44:47], v[180:183], v[12:15], v[44:47]\n"
        "v_cvt_pk_bf16_f32 v9, v34, v35\n"
        "s_waitcnt lgkmcnt(4)\n"
        "v_mfma_f32_16x16x32_bf16 v[40:43], v[184:187], v[16:19], v[40:43]\n"
        "ds_read_b128 v[12:15], v4 offset:12288\n"
        "v_mfma_f32_16x16x32_bf16 v[44:47], v[188:191], v[16:19], v[44:47]\n"
        "v_cvt_pk_bf16_f32 v10, v36, v37\n"
        "s_waitcnt lgkmcnt(4)\n"
        "v_mfma_f32_16x16x32_bf16 v[40:43], v[192:195], v[20:23], v[40:43]\n"
        "ds_read_b128 v[16:19], v3 offset:14336\n"
        "v_mfma_f32_16x16x32_bf16 v[44:47], v[196:199], v[20:23], v[44:47]\n"
        "v_cvt_pk_bf16_f32 v11, v38, v39\n"
        "s_waitcnt lgkmcnt(3)\n"
        "v_mfma_f32_16x16x32_bf16 v[40:43], v[200:203], v[24:27], v[40:43]\n"
        "ds_read_b128 v[20:23], v4 offset:14336\n"
        "v_mfma_f32_16x16x32_bf16 v[44:47], v[204:207], v[24:27], v[44:47]\n"
        "ds_write_b128 v6, v[8:11] offset:12288\n"
        "s_waitcnt lgkmcnt(4)\n"
        "v_mfma_f32_16x16x32_bf16 v[40:43], v[208:211], v[28:31], v[40:43]\n"
        "ds_read_b128 v[24:27], v3 offset:16384\n"
        "v_mfma_f32_16x16x32_bf16 v[44:47], v[212:215], v[28:31], v[44:47]\n"
        "s_waitcnt lgkmcnt(4)\n"
        "v_mfma_f32_16x16x32_bf16 v[40:43], v[216:219], v[12:15], v[40:43]\n"
        "ds_read_b128 v[28:31], v4 offset:16384\n"
        "v_mfma_f32_16x16x32_bf16 v[44:47], v[220:223], v[12:15], v[44:47]\n"
        "s_waitcnt lgkmcnt(2)\n"
        "s_barrier\n"
        "v_add_u32 v3, 18432, v3\n"
        "v_add_u32 v4, 18432, v4\n"
        "s_waitcnt lgkmcnt(4)\n"
        "v_mfma_f32_16x16x32_bf16 v[40:43], v[224:227], v[16:19], v[40:43]\n"
        "ds_read_b128 v[12:15], v3 offset:0\n"
        "v_mfma_f32_16x16x32_bf16 v[44:47], v[228:231], v[16:19], v[44:47]\n"
        "s_waitcnt lgkmcnt(4)\n"
        "v_mfma_f32_16x16x32_bf16 v[40:43], v[232:235], v[20:23], v[40:43]\n"
        "ds_read_b128 v[16:19], v4 offset:0\n"
        "v_mfma_f32_16x16x32_bf16 v[44:47], v[236:239], v[20:23], v[44:47]\n"
        "s_waitcnt lgkmcnt(3)\n"
        "v_mfma_f32_16x16x32_bf16 v[40:43], v[240:243], v[24:27], v[40:43]\n"
        "ds_read_b128 v[20:23], v3 offset:2048\n"
        "v_mfma_f32_16x16x32_bf16 v[44:47], v[244:247], v[24:27], v[44:47]\n"
        "s_waitcnt lgkmcnt(3)\n"
        "v_mfma_f32_16x16x32_bf16 v[40:43], v[248:251], v[28:31], v[40:43]\n"
        "ds_read_b128 v[24:27], v4 offset:2048\n"
        "v_mfma_f32_16x16x32_bf16 v[44:47], v[252:255], v[28:31], v[44:47]\n"
        "s_waitcnt lgkmcnt(3)\n"
        "v_mfma_f32_16x16x32_bf16 v[32:35], v[112:115], v[12:15], 0\n"
        "ds_read_b128 v[28:31], v3 offset:4096\n"
        "v_mfma_f32_16x16x32_bf16 v[36:39], v[116:119], v[12:15], 0\n"
        "s_waitcnt lgkmcnt(3)\n"
        "v_mfma_f32_16x16x32_bf16 v[32:35], v[120:123], v[16:19], v[32:35]\n"
        "ds_read_b128 v[12:15], v4 offset:4096\n"
        "v_mfma_f32_16x16x32_bf16 v[36:39], v[124:127], v[16:19], v[36:39]\n"
        "s_waitcnt lgkmcnt(3)\n"
        "v_mfma_f32_16x16x32_bf16 v[32:35], v[128:131], v[20:23], v[32:35]\n"
        "ds_read_b128 v[16:19], v3 offset:6144\n"
        "v_mfma_f32_16x16x32_bf16 v[36:39], v[132:135], v[20:23], v[36:39]\n"
        "v_cvt_pk_bf16_f32 v8, v64, v65\n"
        "s_waitcnt lgkmcnt(3)\n"
        "v_mfma_f32_16x16x32_bf16 v[32:35], v[136:139], v[24:27], v[32:35]\n"
        "ds_read_b128 v[20:23], v4 offset:6144\n"
        "v_mfma_f32_16x16x32_bf16 v[36:39], v[140:143], v[24:27], v[36:39]\n"
        "v_cvt_pk_bf16_f32 v9, v66, v67\n"
        "s_waitcnt lgkmcnt(3)\n"
        "v_mfma_f32_16x16x32_bf16 v[32:35], v[144:147], v[28:31], v[32:35]\n"
        "ds_read_b128 v[24:27], v3 offset:8192\n"
        "v_mfma_f32_16x16x32_bf16 v[36:39], v[148:151], v[28:31], v[36:39]\n"
        "v_cvt_pk_bf16_f32 v10, v68, v69\n"
        "s_waitcnt lgkmcnt(3)\n"
        "v_mfma_f32_16x16x32_bf16 v[32:35], v[152:155], v[12:15], v[32:35]\n"
        "ds_read_b128 v[28:31], v4 offset:8192\n"
        "v_mfma_f32_16x16x32_bf16 v[36:39], v[156:159], v[12:15], v[36:39]\n"
        "v_cvt_pk_bf16_f32 v11, v70, v71\n"
        "s_waitcnt lgkmcnt(3)\n"
        "v_mfma_f32_16x16x32_bf16 v[32:35], v[160:163], v[16:19], v[32:35]\n"
        "ds_read_b128 v[12:15], v3 offset:10240\n"
        "v_mfma_f32_16x16x32_bf16 v[36:39], v[164:167], v[16:19], v[36:39]\n"
        "ds_write_b128 v6, v[8:11] offset:16384\n"
        "s_waitcnt lgkmcnt(4)\n"
        "v_mfma_f32_16x16x32_bf16 v[32:35], v[168:171], v[20:23], v[32:35]\n"
        "ds_read_b128 v[16:19], v4 offset:10240\n"
        "v_mfma_f32_16x16x32_bf16 v[36:39], v[172:175], v[20:23], v[36:39]\n"
        "v_cvt_pk_bf16_f32 v8, v40, v41\n"
        "s_waitcnt lgkmcnt(4)\n"
        "v_mfma_f32_16x16x32_bf16 v[32:35], v[176:179], v[24:27], v[32:35]\n"
        "ds_read_b128 v[20:23], v3 offset:12288\n"
        "v_mfma_f32_16x16x32_bf16 v[36:39], v[180:183], v[24:27], v[36:39]\n"
        "v_cvt_pk_bf16_f32 v9, v42, v43\n"
        "s_waitcnt lgkmcnt(4)\n"
        "v_mfma_f32_16x16x32_bf16 v[32:35], v[184:187], v[28:31], v[32:35]\n"
        "ds_read_b128 v[24:27], v4 offset:12288\n"
        "v_mfma_f32_16x16x32_bf16 v[36:39], v[188:191], v[28:31], v[36:39]\n"
        "v_cvt_pk_bf16_f32 v10, v44, v45\n"
        "s_waitcnt lgkmcnt(4)\n"
        "v_mfma_f32_16x16x32_bf16 v[32:35], v[192:195], v[12:15], v[32:35]\n"
        "ds_read_b128 v[28:31], v3 offset:14336\n"
        "v_mfma_f32_16x16x32_bf16 v[36:39], v[196:199], v[12:15], v[36:39]\n"
        "v_cvt_pk_bf16_f32 v11, v46, v47\n"
        "s_waitcnt lgkmcnt(3)\n"
        "v_mfma_f32_16x16x32_bf16 v[32:35], v[200:203], v[16:19], v[32:35]\n"
        "ds_read_b128 v[12:15], v4 offset:14336\n"
        "v_mfma_f32_16x16x32_bf16 v[36:39], v[204:207], v[16:19], v[36:39]\n"
        "ds_write_b128 v6, v[8:11] offset:20480\n"
        "s_waitcnt lgkmcnt(4)\n"
        "v_mfma_f32_16x16x32_bf16 v[32:35], v[208:211], v[20:23], v[32:35]\n"
        "ds_read_b128 v[16:19], v3 offset:16384\n"
        "v_mfma_f32_16x16x32_bf16 v[36:39], v[212:215], v[20:23], v[36:39]\n"
        "s_waitcnt lgkmcnt(4)\n"
        "v_mfma_f32_16x16x32_bf16 v[32:35], v[216:219], v[24:27], v[32:35]\n"
        "ds_read_b128 v[20:23], v4 offset:16384\n"
        "v_mfma_f32_16x16x32_bf16 v[36:39], v[220:223], v[24:27], v[36:39]\n"
        "s_waitcnt lgkmcnt(2)\n"
        "s_barrier\n"
        "v_subrev_u32 v3, 140544, v3\n"
        "v_subrev_u32 v4, 140544, v4\n"
        "s_waitcnt lgkmcnt(4)\n"
        "v_mfma_f32_16x16x32_bf16 v[32:35], v[224:227], v[28:31], v[32:35]\n"
        "ds_read_b128 v[24:27], v3 offset:0\n"
        "v_mfma_f32_16x16x32_bf16 v[36:39], v[228:231], v[28:31], v[36:39]\n"
        "s_waitcnt lgkmcnt(4)\n"
        "v_mfma_f32_16x16x32_bf16 v[32:35], v[232:235], v[12:15], v[32:35]\n"
        "ds_read_b128 v[28:31], v4 offset:0\n"
        "v_mfma_f32_16x16x32_bf16 v[36:39], v[236:239], v[12:15], v[36:39]\n"
        "s_waitcnt lgkmcnt(3)\n"
        "v_mfma_f32_16x16x32_bf16 v[32:35], v[240:243], v[16:19], v[32:35]\n"
        "ds_read_b128 v[12:15], v3 offset:2048\n"
        "v_mfma_f32_16x16x32_bf16 v[36:39], v[244:247], v[16:19], v[36:39]\n"
        "s_waitcnt lgkmcnt(3)\n"
        "v_mfma_f32_16x16x32_bf16 v[32:35], v[248:251], v[20:23], v[32:35]\n"
        "ds_read_b128 v[16:19], v4 offset:2048\n"
        "v_mfma_f32_16x16x32_bf16 v[36:39], v[252:255], v[20:23], v[36:39]\n"
        "s_waitcnt lgkmcnt(3)\n"
        "v_mfma_f32_16x16x32_bf16 v[40:43], v[112:115], v[24:27], 0\n"
        "ds_read_b128 v[20:23], v3 offset:4096\n"
        "v_mfma_f32_16x16x32_bf16 v[44:47], v[116:119], v[24:27], 0\n"
        "s_waitcnt lgkmcnt(3)\n"
        "v_mfma_f32_16x16x32_bf16 v[40:43], v[120:123], v[28:31], v[40:43]\n"
        "ds_read_b128 v[24:27], v4 offset:4096\n"
        "v_mfma_f32_16x16x32_bf16 v[44:47], v[124:127], v[28:31], v[44:47]\n"
        "s_waitcnt lgkmcnt(3)\n"
        "v_mfma_f32_16x16x32_bf16 v[40:43], v[128:131], v[12:15], v[40:43]\n"
        "ds_read_b128 v[28:31], v3 offset:6144\n"
        "v_mfma_f32_16x16x32_bf16 v[44:47], v[132:135], v[12:15], v[44:47]\n"
        "v_cvt_pk_bf16_f32 v8, v72, v73\n"
        "s_waitcnt lgkmcnt(3)\n"
        "v_mfma_f32_16x16x32_bf16 v[40:43], v[136:139], v[16:19], v[40:43]\n"
        "ds_read_b128 v[12:15], v4 offset:6144\n"
        "v_mfma_f32_16x16x32_bf16 v[44:47], v[140:143], v[16:19], v[44:47]\n"
        "v_cvt_pk_bf16_f32 v9, v74, v75\n"
        "s_waitcnt lgkmcnt(3)\n"
        "v_mfma_f32_16x16x32_bf16 v[40:43], v[144:147], v[20:23], v[40:43]\n"
        "ds_read_b128 v[16:19], v3 offset:8192\n"
        "v_mfma_f32_16x16x32_bf16 v[44:47], v[148:151], v[20:23], v[44:47]\n"
        "v_cvt_pk_bf16_f32 v10, v76, v77\n"
        "s_waitcnt lgkmcnt(3)\n"
        "v_mfma_f32_16x16x32_bf16 v[40:43], v[152:155], v[24:27], v[40:43]\n"
        "ds_read_b128 v[20:23], v4 offset:8192\n"
        "v_mfma_f32_16x16x32_bf16 v[44:47], v[156:159], v[24:27], v[44:47]\n"
        "v_cvt_pk_bf16_f32 v11, v78, v79\n"
        "s_waitcnt lgkmcnt(3)\n"
        "v_mfma_f32_16x16x32_bf16 v[40:43], v[160:163], v[28:31], v[40:43]\n"
        "ds_read_b128 v[24:27], v3 offset:10240\n"
        "v_mfma_f32_16x16x32_bf16 v[44:47], v[164:167], v[28:31], v[44:47]\n"
        "ds_write_b128 v6, v[8:11] offset:24576\n"
        "s_waitcnt lgkmcnt(4)\n"
        "v_mfma_f32_16x16x32_bf16 v[40:43], v[168:171], v[12:15], v[40:43]\n"
        "ds_read_b128 v[28:31], v4 offset:10240\n"
        "v_mfma_f32_16x16x32_bf16 v[44:47], v[172:175], v[12:15], v[44:47]\n"
        "v_cvt_pk_bf16_f32 v8, v32, v33\n"
        "s_waitcnt lgkmcnt(4)\n"
        "v_mfma_f32_16x16x32_bf16 v[40:43], v[176:179], v[16:19], v[40:43]\n"
        "ds_read_b128 v[12:15], v3 offset:12288\n"
        "v_mfma_f32_16x16x32_bf16 v[44:47], v[180:183], v[16:19], v[44:47]\n"
        "v_cvt_pk_bf16_f32 v9, v34, v35\n"
        "s_waitcnt lgkmcnt(4)\n"
        "v_mfma_f32_16x16x32_bf16 v[40:43], v[184:187], v[20:23], v[40:43]\n"
        "ds_read_b128 v[16:19], v4 offset:12288\n"
        "v_mfma_f32_16x16x32_bf16 v[44:47], v[188:191], v[20:23], v[44:47]\n"
        "v_cvt_pk_bf16_f32 v10, v36, v37\n"
        "s_waitcnt lgkmcnt(4)\n"
        "v_mfma_f32_16x16x32_bf16 v[40:43], v[192:195], v[24:27], v[40:43]\n"
        "ds_read_b128 v[20:23], v3 offset:14336\n"
        "v_mfma_f32_16x16x32_bf16 v[44:47], v[196:199], v[24:27], v[44:47]\n"
        "v_cvt_pk_bf16_f32 v11, v38, v39\n"
        "s_waitcnt lgkmcnt(3)\n"
        "v_mfma_f32_16x16x32_bf16 v[40:43], v[200:203], v[28:31], v[40:43]\n"
        "ds_read_b128 v[24:27], v4 offset:14336\n"
        "v_mfma_f32_16x16x32_bf16 v[44:47], v[204:207], v[28:31], v[44:47]\n"
        "ds_write_b128 v6, v[8:11] offset:28672\n"
        "s_waitcnt lgkmcnt(4)\n"
        "v_mfma_f32_16x16x32_bf16 v[40:43], v[208:211], v[12:15], v[40:43]\n"
        "ds_read_b128 v[28:31], v3 offset:16384\n"
        "v_mfma_f32_16x16x32_bf16 v[44:47], v[212:215], v[12:15], v[44:47]\n"
        "s_waitcnt lgkmcnt(4)\n"
        "v_mfma_f32_16x16x32_bf16 v[40:43], v[216:219], v[16:19], v[40:43]\n"
        "ds_read_b128 v[12:15], v4 offset:16384\n"
        "v_mfma_f32_16x16x32_bf16 v[44:47], v[220:223], v[16:19], v[44:47]\n"
        "s_waitcnt lgkmcnt(2)\n"
        "s_barrier\n"
        "v_add_u32 v3, 18432, v3\n"
        "v_add_u32 v4, 18432, v4\n"
        "s_waitcnt lgkmcnt(4)\n"
        "v_mfma_f32_16x16x32_bf16 v[40:43], v[224:227], v[20:23], v[40:43]\n"
        "ds_read_b128 v[16:19], v3 offset:0\n"
        "v_mfma_f32_16x16x32_bf16 v[44:47], v[228:231], v[20:23], v[44:47]\n"
        "s_waitcnt lgkmcnt(4)\n"
        "v_mfma_f32_16x16x32_bf16 v[40:43], v[232:235], v[24:27], v[40:43]\n"
        "ds_read_b128 v[20:23], v4 offset:0\n"
        "v_mfma_f32_16x16x32_bf16 v[44:47], v[236:239], v[24:27], v[44:47]\n"
        "s_waitcnt lgkmcnt(3)\n"
        "v_mfma_f32_16x16x32_bf16 v[40:43], v[240:243], v[28:31], v[40:43]\n"
        "ds_read_b128 v[24:27], v3 offset:2048\n"
        "v_mfma_f32_16x16x32_bf16 v[44:47], v[244:247], v[28:31], v[44:47]\n"
        "s_waitcnt lgkmcnt(3)\n"
        "v_mfma_f32_16x16x32_bf16 v[40:43], v[248:251], v[12:15], v[40:43]\n"
        "ds_read_b128 v[28:31], v4 offset:2048\n"
        "v_mfma_f32_16x16x32_bf16 v[44:47], v[252:255], v[12:15], v[44:47]\n"
        "s_waitcnt lgkmcnt(3)\n"
        "v_mfma_f32_16x16x32_bf16 v[32:35], v[112:115], v[16:19], 0\n"
        "ds_read_b128 v[12:15], v3 offset:4096\n"
        "v_mfma_f32_16x16x32_bf16 v[36:39], v[116:119], v[16:19], 0\n"
        "s_waitcnt lgkmcnt(3)\n"
        "v_mfma_f32_16x16x32_bf16 v[32:35], v[120:123], v[20:23], v[32:35]\n"
        "ds_read_b128 v[16:19], v4 offset:4096\n"
        "v_mfma_f32_16x16x32_bf16 v[36:39], v[124:127], v[20:23], v[36:39]\n"
        "s_waitcnt lgkmcnt(3)\n"
        "v_mfma_f32_16x16x32_bf16 v[32:35], v[128:131], v[24:27], v[32:35]\n"
        "ds_read_b128 v[20:23], v3 offset:6144\n"
        "v_mfma_f32_16x16x32_bf16 v[36:39], v[132:135], v[24:27], v[36:39]\n"
        "v_cvt_pk_bf16_f32 v8, v80, v81\n"
        "s_waitcnt lgkmcnt(3)\n"
        "v_mfma_f32_16x16x32_bf16 v[32:35], v[136:139], v[28:31], v[32:35]\n"
        "ds_read_b128 v[24:27], v4 offset:6144\n"
        "v_mfma_f32_16x16x32_bf16 v[36:39], v[140:143], v[28:31], v[36:39]\n"
        "v_cvt_pk_bf16_f32 v9, v82, v83\n"
        "s_waitcnt lgkmcnt(3)\n"
        "v_mfma_f32_16x16x32_bf16 v[32:35], v[144:147], v[12:15], v[32:35]\n"
        "ds_read_b128 v[28:31], v3 offset:8192\n"
        "v_mfma_f32_16x16x32_bf16 v[36:39], v[148:151], v[12:15], v[36:39]\n"
        "v_cvt_pk_bf16_f32 v10, v84, v85\n"
        "s_waitcnt lgkmcnt(3)\n"
        "v_mfma_f32_16x16x32_bf16 v[32:35], v[152:155], v[16:19], v[32:35]\n"
        "ds_read_b128 v[12:15], v4 offset:8192\n"
        "v_mfma_f32_16x16x32_bf16 v[36:39], v[156:159], v[16:19], v[36:39]\n"
        "v_cvt_pk_bf16_f32 v11, v86, v87\n"
        "s_waitcnt lgkmcnt(3)\n"
        "v_mfma_f32_16x16x32_bf16 v[32:35], v[160:163], v[20:23], v[32:35]\n"
        "ds_read_b128 v[16:19], v3 offset:10240\n"
        "v_mfma_f32_16x16x32_bf16 v[36:39], v[164:167], v[20:23], v[36:39]\n"
        "ds_write_b128 v6, v[8:11] offset:32768\n"
        "s_waitcnt lgkmcnt(4)\n"
        "v_mfma_f32_16x16x32_bf16 v[32:35], v[168:171], v[24:27], v[32:35]\n"
        "ds_read_b128 v[20:23], v4 offset:10240\n"
        "v_mfma_f32_16x16x32_bf16 v[36:39], v[172:175], v[24:27], v[36:39]\n"
        "v_cvt_pk_bf16_f32 v8, v40, v41\n"
        "s_waitcnt lgkmcnt(4)\n"
        "v_mfma_f32_16x16x32_bf16 v[32:35], v[176:179], v[28:31], v[32:35]\n"
        "ds_read_b128 v[24:27], v3 offset:12288\n"
        "v_mfma_f32_16x16x32_bf16 v[36:39], v[180:183], v[28:31], v[36:39]\n"
        "v_cvt_pk_bf16_f32 v9, v42, v43\n"
        "s_waitcnt lgkmcnt(4)\n"
        "v_mfma_f32_16x16x32_bf16 v[32:35], v[184:187], v[12:15], v[32:35]\n"
        "ds_read_b128 v[28:31], v4 offset:12288\n"
        "v_mfma_f32_16x16x32_bf16 v[36:39], v[188:191], v[12:15], v[36:39]\n"
        "v_cvt_pk_bf16_f32 v10, v44, v45\n"
        "s_waitcnt lgkmcnt(4)\n"
        "v_mfma_f32_16x16x32_bf16 v[32:35], v[192:195], v[16:19], v[32:35]\n"
        "ds_read_b128 v[12:15], v3 offset:14336\n"
        "v_mfma_f32_16x16x32_bf16 v[36:39], v[196:199], v[16:19], v[36:39]\n"
        "v_cvt_pk_bf16_f32 v11, v46, v47\n"
        "s_waitcnt lgkmcnt(3)\n"
        "v_mfma_f32_16x16x32_bf16 v[32:35], v[200:203], v[20:23], v[32:35]\n"
        "ds_read_b128 v[16:19], v4 offset:14336\n"
        "v_mfma_f32_16x16x32_bf16 v[36:39], v[204:207], v[20:23], v[36:39]\n"
        "ds_write_b128 v6, v[8:11] offset:36864\n"
        "s_waitcnt lgkmcnt(4)\n"
        "v_mfma_f32_16x16x32_bf16 v[32:35], v[208:211], v[24:27], v[32:35]\n"
        "ds_read_b128 v[20:23], v3 offset:16384\n"
        "v_mfma_f32_16x16x32_bf16 v[36:39], v[212:215], v[24:27], v[36:39]\n"
        "s_waitcnt lgkmcnt(4)\n"
        "v_mfma_f32_16x16x32_bf16 v[32:35], v[216:219], v[28:31], v[32:35]\n"
        "ds_read_b128 v[24:27], v4 offset:16384\n"
        "v_mfma_f32_16x16x32_bf16 v[36:39], v[220:223], v[28:31], v[36:39]\n"
        "s_waitcnt lgkmcnt(2)\n"
        "s_barrier\n"
        "v_add_u32 v3, 85248, v3\n"
        "v_add_u32 v4, 85248, v4\n"
        "s_waitcnt lgkmcnt(4)\n"
        "v_mfma_f32_16x16x32_bf16 v[32:35], v[224:227], v[12:15], v[32:35]\n"
        "ds_read_b128 v[28:31], v3 offset:0\n"
        "v_mfma_f32_16x16x32_bf16 v[36:39], v[228:231], v[12:15], v[36:39]\n"
        "s_waitcnt lgkmcnt(4)\n"
        "v_mfma_f32_16x16x32_bf16 v[32:35], v[232:235], v[16:19], v[32:35]\n"
        "ds_read_b128 v[12:15], v4 offset:0\n"
        "v_mfma_f32_16x16x32_bf16 v[36:39], v[236:239], v[16:19], v[36:39]\n"
        "s_waitcnt lgkmcnt(3)\n"
        "v_mfma_f32_16x16x32_bf16 v[32:35], v[240:243], v[20:23], v[32:35]\n"
        "ds_read_b128 v[16:19], v3 offset:2048\n"
        "v_mfma_f32_16x16x32_bf16 v[36:39], v[244:247], v[20:23], v[36:39]\n"
        "s_waitcnt lgkmcnt(3)\n"
        "v_mfma_f32_16x16x32_bf16 v[32:35], v[248:251], v[24:27], v[32:35]\n"
        "ds_read_b128 v[20:23], v4 offset:2048\n"
        "v_mfma_f32_16x16x32_bf16 v[36:39], v[252:255], v[24:27], v[36:39]\n"
        "s_waitcnt lgkmcnt(3)\n"
        "v_mfma_f32_16x16x32_bf16 v[40:43], v[112:115], v[28:31], 0\n"
        "ds_read_b128 v[24:27], v3 offset:4096\n"
        "v_mfma_f32_16x16x32_bf16 v[44:47], v[116:119], v[28:31], 0\n"
        "s_waitcnt lgkmcnt(3)\n"
        "v_mfma_f32_16x16x32_bf16 v[40:43], v[120:123], v[12:15], v[40:43]\n"
        "ds_read_b128 v[28:31], v4 offset:4096\n"
        "v_mfma_f32_16x16x32_bf16 v[44:47], v[124:127], v[12:15], v[44:47]\n"
        "s_waitcnt lgkmcnt(3)\n"
        "v_mfma_f32_16x16x32_bf16 v[40:43], v[128:131], v[16:19], v[40:43]\n"
        "ds_read_b128 v[12:15], v3 offset:6144\n"
        "v_mfma_f32_16x16x32_bf16 v[44:47], v[132:135], v[16:19], v[44:47]\n"
        "v_cvt_pk_bf16_f32 v8, v88, v89\n"
        "s_waitcnt lgkmcnt(3)\n"
        "v_mfma_f32_16x16x32_bf16 v[40:43], v[136:139], v[20:23], v[40:43]\n"
        "ds_read_b128 v[16:19], v4 offset:6144\n"
        "v_mfma_f32_16x16x32_bf16 v[44:47], v[140:143], v[20:23], v[44:47]\n"
        "v_cvt_pk_bf16_f32 v9, v90, v91\n"
        "s_waitcnt lgkmcnt(3)\n"
        "v_mfma_f32_16x16x32_bf16 v[40:43], v[144:147], v[24:27], v[40:43]\n"
        "ds_read_b128 v[20:23], v3 offset:8192\n"
        "v_mfma_f32_16x16x32_bf16 v[44:47], v[148:151], v[24:27], v[44:47]\n"
        "v_cvt_pk_bf16_f32 v10, v92, v93\n"
        "s_waitcnt lgkmcnt(3)\n"
        "v_mfma_f32_16x16x32_bf16 v[40:43], v[152:155], v[28:31], v[40:43]\n"
        "ds_read_b128 v[24:27], v4 offset:8192\n"
        "v_mfma_f32_16x16x32_bf16 v[44:47], v[156:159], v[28:31], v[44:47]\n"
        "v_cvt_pk_bf16_f32 v11, v94, v95\n"
        "s_waitcnt lgkmcnt(3)\n"
        "v_mfma_f32_16x16x32_bf16 v[40:43], v[160:163], v[12:15], v[40:43]\n"
        "ds_read_b128 v[28:31], v3 offset:10240\n"
        "v_mfma_f32_16x16x32_bf16 v[44:47], v[164:167], v[12:15], v[44:47]\n"
        "ds_write_b128 v6, v[8:11] offset:40960\n"
        "s_waitcnt lgkmcnt(4)\n"
        "v_mfma_f32_16x16x32_bf16 v[40:43], v[168:171], v[16:19], v[40:43]\n"
        "ds_read_b128 v[12:15], v4 offset:10240\n"
        "v_mfma_f32_16x16x32_bf16 v[44:47], v[172:175], v[16:19], v[44:47]\n"
        "v_cvt_pk_bf16_f32 v8, v32, v33\n"
        "s_waitcnt lgkmcnt(4)\n"
        "v_mfma_f32_16x16x32_bf16 v[40:43], v[176:179], v[20:23], v[40:43]\n"
        "ds_read_b128 v[16:19], v3 offset:12288\n"
        "v_mfma_f32_16x16x32_bf16 v[44:47], v[180:183], v[20:23], v[44:47]\n"
        "v_cvt_pk_bf16_f32 v9, v34, v35\n"
        "s_waitcnt lgkmcnt(4)\n"
        "v_mfma_f32_16x16x32_bf16 v[40:43], v[184:187], v[24:27], v[40:43]\n"
        "ds_read_b128 v[20:23], v4 offset:12288\n"
        "v_mfma_f32_16x16x32_bf16 v[44:47], v[188:191], v[24:27], v[44:47]\n"
        "v_cvt_pk_bf16_f32 v10, v36, v37\n"
        "s_waitcnt lgkmcnt(4)\n"
        "v_mfma_f32_16x16x32_bf16 v[40:43], v[192:195], v[28:31], v[40:43]\n"
        "ds_read_b128 v[24:27], v3 offset:14336\n"
        "v_mfma_f32_16x16x32_bf16 v[44:47], v[196:199], v[28:31], v[44:47]\n"
        "v_cvt_pk_bf16_f32 v11, v38, v39\n"
        "s_waitcnt lgkmcnt(3)\n"
        "v_mfma_f32_16x16x32_bf16 v[40:43], v[200:203], v[12:15], v[40:43]\n"
        "ds_read_b128 v[28:31], v4 offset:14336\n"
        "v_mfma_f32_16x16x32_bf16 v[44:47], v[204:207], v[12:15], v[44:47]\n"
        "ds_write_b128 v6, v[8:11] offset:45056\n"
        "s_waitcnt lgkmcnt(4)\n"
        "v_mfma_f32_16x16x32_bf16 v[40:43], v[208:211], v[16:19], v[40:43]\n"
        "ds_read_b128 v[12:15], v3 offset:16384\n"
        "v_mfma_f32_16x16x32_bf16 v[44:47], v[212:215], v[16:19], v[44:47]\n"
        "s_waitcnt lgkmcnt(4)\n"
        "v_mfma_f32_16x16x32_bf16 v[40:43], v[216:219], v[20:23], v[40:43]\n"
        "ds_read_b128 v[16:19], v4 offset:16384\n"
        "v_mfma_f32_16x16x32_bf16 v[44:47], v[220:223], v[20:23], v[44:47]\n"
        "s_waitcnt lgkmcnt(2)\n"
        "s_barrier\n"
        "v_add_u32 v3, 18432, v3\n"
        "v_add_u32 v4, 18432, v4\n"
        "s_waitcnt lgkmcnt(4)\n"
        "v_mfma_f32_16x16x32_bf16 v[40:43], v[224:227], v[24:27], v[40:43]\n"
        "ds_read_b128 v[20:23], v3 offset:0\n"
        "v_mfma_f32_16x16x32_bf16 v[44:47], v[228:231], v[24:27], v[44:47]\n"
        "s_waitcnt lgkmcnt(4)\n"
        "v_mfma_f32_16x16x32_bf16 v[40:43], v[232:235], v[28:31], v[40:43]\n"
        "ds_read_b128 v[24:27], v4 offset:0\n"
        "v_mfma_f32_16x16x32_bf16 v[44:47], v[236:239], v[28:31], v[44:47]\n"
        "s_waitcnt lgkmcnt(3)\n"
        "v_mfma_f32_16x16x32_bf16 v[40:43], v[240:243], v[12:15], v[40:43]\n"
        "ds_read_b128 v[28:31], v3 offset:2048\n"
        "v_mfma_f32_16x16x32_bf16 v[44:47], v[244:247], v[12:15], v[44:47]\n"
        "s_waitcnt lgkmcnt(3)\n"
        "v_mfma_f32_16x16x32_bf16 v[40:43], v[248:251], v[16:19], v[40:43]\n"
        "ds_read_b128 v[12:15], v4 offset:2048\n"
        "v_mfma_f32_16x16x32_bf16 v[44:47], v[252:255], v[16:19], v[44:47]\n"
        "s_waitcnt lgkmcnt(3)\n"
        "v_mfma_f32_16x16x32_bf16 v[32:35], v[112:115], v[20:23], 0\n"
        "ds_read_b128 v[16:19], v3 offset:4096\n"
        "v_mfma_f32_16x16x32_bf16 v[36:39], v[116:119], v[20:23], 0\n"
        "s_waitcnt lgkmcnt(3)\n"
        "v_mfma_f32_16x16x32_bf16 v[32:35], v[120:123], v[24:27], v[32:35]\n"
        "ds_read_b128 v[20:23], v4 offset:4096\n"
        "v_mfma_f32_16x16x32_bf16 v[36:39], v[124:127], v[24:27], v[36:39]\n"
        "s_waitcnt lgkmcnt(3)\n"
        "v_mfma_f32_16x16x32_bf16 v[32:35], v[128:131], v[28:31], v[32:35]\n"
        "ds_read_b128 v[24:27], v3 offset:6144\n"
        "v_mfma_f32_16x16x32_bf16 v[36:39], v[132:135], v[28:31], v[36:39]\n"
        "v_cvt_pk_bf16_f32 v8, v96, v97\n"
        "s_waitcnt lgkmcnt(3)\n"
        "v_mfma_f32_16x16x32_bf16 v[32:35], v[136:139], v[12:15], v[32:35]\n"
        "ds_read_b128 v[28:31], v4 offset:6144\n"
        "v_mfma_f32_16x16x32_bf16 v[36:39], v[140:143], v[12:15], v[36:39]\n"
        "v_cvt_pk_bf16_f32 v9, v98, v99\n"
        "s_waitcnt lgkmcnt(3)\n"
        "v_mfma_f32_16x16x32_bf16 v[32:35], v[144:147], v[16:19], v[32:35]\n"
        "ds_read_b128 v[12:15], v3 offset:8192\n"
        "v_mfma_f32_16x16x32_bf16 v[36:39], v[148:151], v[16:19], v[36:39]\n"
        "v_cvt_pk_bf16_f32 v10, v100, v101\n"
        "s_waitcnt lgkmcnt(3)\n"
        "v_mfma_f32_16x16x32_bf16 v[32:35], v[152:155], v[20:23], v[32:35]\n"
        "ds_read_b128 v[16:19], v4 offset:8192\n"
        "v_mfma_f32_16x16x32_bf16 v[36:39], v[156:159], v[20:23], v[36:39]\n"
        "v_cvt_pk_bf16_f32 v11, v102, v103\n"
        "s_waitcnt lgkmcnt(3)\n"
        "v_mfma_f32_16x16x32_bf16 v[32:35], v[160:163], v[24:27], v[32:35]\n"
        "ds_read_b128 v[20:23], v3 offset:10240\n"
        "v_mfma_f32_16x16x32_bf16 v[36:39], v[164:167], v[24:27], v[36:39]\n"
        "ds_write_b128 v6, v[8:11] offset:0\n"
        "s_waitcnt lgkmcnt(4)\n"
        "v_mfma_f32_16x16x32_bf16 v[32:35], v[168:171], v[28:31], v[32:35]\n"
        "ds_read_b128 v[24:27], v4 offset:10240\n"
        "v_mfma_f32_16x16x32_bf16 v[36:39], v[172:175], v[28:31], v[36:39]\n"
        "v_cvt_pk_bf16_f32 v8, v40, v41\n"
        "s_waitcnt lgkmcnt(4)\n"
        "v_mfma_f32_16x16x32_bf16 v[32:35], v[176:179], v[12:15], v[32:35]\n"
        "ds_read_b128 v[28:31], v3 offset:12288\n"
        "v_mfma_f32_16x16x32_bf16 v[36:39], v[180:183], v[12:15], v[36:39]\n"
        "v_cvt_pk_bf16_f32 v9, v42, v43\n"
        "s_waitcnt lgkmcnt(4)\n"
        "v_mfma_f32_16x16x32_bf16 v[32:35], v[184:187], v[16:19], v[32:35]\n"
        "ds_read_b128 v[12:15], v4 offset:12288\n"
        "v_mfma_f32_16x16x32_bf16 v[36:39], v[188:191], v[16:19], v[36:39]\n"
        "v_cvt_pk_bf16_f32 v10, v44, v45\n"
        "s_waitcnt lgkmcnt(4)\n"
        "v_mfma_f32_16x16x32_bf16 v[32:35], v[192:195], v[20:23], v[32:35]\n"
        "ds_read_b128 v[16:19], v3 offset:14336\n"
        "v_mfma_f32_16x16x32_bf16 v[36:39], v[196:199], v[20:23], v[36:39]\n"
        "v_cvt_pk_bf16_f32 v11, v46, v47\n"
        "s_waitcnt lgkmcnt(3)\n"
        "v_mfma_f32_16x16x32_bf16 v[32:35], v[200:203], v[24:27], v[32:35]\n"
        "ds_read_b128 v[20:23], v4 offset:14336\n"
        "v_mfma_f32_16x16x32_bf16 v[36:39], v[204:207], v[24:27], v[36:39]\n"
        "ds_write_b128 v6, v[8:11] offset:4096\n"
        "s_waitcnt lgkmcnt(4)\n"
        "v_mfma_f32_16x16x32_bf16 v[32:35], v[208:211], v[28:31], v[32:35]\n"
        "ds_read_b128 v[24:27], v3 offset:16384\n"
        "v_mfma_f32_16x16x32_bf16 v[36:39], v[212:215], v[28:31], v[36:39]\n"
        "s_waitcnt lgkmcnt(4)\n"
        "v_mfma_f32_16x16x32_bf16 v[32:35], v[216:219], v[12:15], v[32:35]\n"
        "ds_read_b128 v[28:31], v4 offset:16384\n"
        "v_mfma_f32_16x16x32_bf16 v[36:39], v[220:223], v[12:15], v[36:39]\n"
        "s_waitcnt lgkmcnt(2)\n"
        "s_barrier\n"
        "v_add_u32 v3, 18432, v3\n"
        "v_add_u32 v4, 18432, v4\n"
        "s_waitcnt lgkmcnt(4)\n"
        "v_mfma_f32_16x16x32_bf16 v[32:35], v[224:227], v[16:19], v[32:35]\n"
        "ds_read_b128 v[12:15], v3 offset:0\n"
        "v_mfma_f32_16x16x32_bf16 v[36:39], v[228:231], v[16:19], v[36:39]\n"
        "s_waitcnt lgkmcnt(4)\n"
        "v_mfma_f32_16x16x32_bf16 v[32:35], v[232:235], v[20:23], v[32:35]\n"
        "ds_read_b128 v[16:19], v4 offset:0\n"
        "v_mfma_f32_16x16x32_bf16 v[36:39], v[236:239], v[20:23], v[36:39]\n"
        "s_waitcnt lgkmcnt(3)\n"
        "v_mfma_f32_16x16x32_bf16 v[32:35], v[240:243], v[24:27], v[32:35]\n"
        "ds_read_b128 v[20:23], v3 offset:2048\n"
        "v_mfma_f32_16x16x32_bf16 v[36:39], v[244:247], v[24:27], v[36:39]\n"
        "s_waitcnt lgkmcnt(3)\n"
        "v_mfma_f32_16x16x32_bf16 v[32:35], v[248:251], v[28:31], v[32:35]\n"
        "ds_read_b128 v[24:27], v4 offset:2048\n"
        "v_mfma_f32_16x16x32_bf16 v[36:39], v[252:255], v[28:31], v[36:39]\n"
        "s_waitcnt lgkmcnt(3)\n"
        "v_mfma_f32_16x16x32_bf16 v[40:43], v[112:115], v[12:15], 0\n"
        "ds_read_b128 v[28:31], v3 offset:4096\n"
        "v_mfma_f32_16x16x32_bf16 v[44:47], v[116:119], v[12:15], 0\n"
        "s_waitcnt lgkmcnt(3)\n"
        "v_mfma_f32_16x16x32_bf16 v[40:43], v[120:123], v[16:19], v[40:43]\n"
        "ds_read_b128 v[12:15], v4 offset:4096\n"
        "v_mfma_f32_16x16x32_bf16 v[44:47], v[124:127], v[16:19], v[44:47]\n"
        "s_waitcnt lgkmcnt(3)\n"
        "v_mfma_f32_16x16x32_bf16 v[40:43], v[128:131], v[20:23], v[40:43]\n"
        "ds_read_b128 v[16:19], v3 offset:6144\n"
        "v_mfma_f32_16x16x32_bf16 v[44:47], v[132:135], v[20:23], v[44:47]\n"
        "v_cvt_pk_bf16_f32 v8, v104, v105\n"
        "s_waitcnt lgkmcnt(3)\n"
        "v_mfma_f32_16x16x32_bf16 v[40:43], v[136:139], v[24:27], v[40:43]\n"
        "ds_read_b128 v[20:23], v4 offset:6144\n"
        "v_mfma_f32_16x16x32_bf16 v[44:47], v[140:143], v[24:27], v[44:47]\n"
        "v_cvt_pk_bf16_f32 v9, v106, v107\n"
        "s_waitcnt lgkmcnt(3)\n"
        "v_mfma_f32_16x16x32_bf16 v[40:43], v[144:147], v[28:31], v[40:43]\n"
        "ds_read_b128 v[24:27], v3 offset:8192\n"
        "v_mfma_f32_16x16x32_bf16 v[44:47], v[148:151], v[28:31], v[44:47]\n"
        "v_cvt_pk_bf16_f32 v10, v108, v109\n"
        "s_waitcnt lgkmcnt(3)\n"
        "v_mfma_f32_16x16x32_bf16 v[40:43], v[152:155], v[12:15], v[40:43]\n"
        "ds_read_b128 v[28:31], v4 offset:8192\n"
        "v_mfma_f32_16x16x32_bf16 v[44:47], v[156:159], v[12:15], v[44:47]\n"
        "v_cvt_pk_bf16_f32 v11, v110, v111\n"
        "s_waitcnt lgkmcnt(3)\n"
        "v_mfma_f32_16x16x32_bf16 v[40:43], v[160:163], v[16:19], v[40:43]\n"
        "ds_read_b128 v[12:15], v3 offset:10240\n"
        "v_mfma_f32_16x16x32_bf16 v[44:47], v[164:167], v[16:19], v[44:47]\n"
        "ds_write_b128 v6, v[8:11] offset:8192\n"
        "s_waitcnt lgkmcnt(4)\n"
        "v_mfma_f32_16x16x32_bf16 v[40:43], v[168:171], v[20:23], v[40:43]\n"
        "ds_read_b128 v[16:19], v4 offset:10240\n"
        "v_mfma_f32_16x16x32_bf16 v[44:47], v[172:175], v[20:23], v[44:47]\n"
        "v_cvt_pk_bf16_f32 v8, v32, v33\n"
        "s_waitcnt lgkmcnt(4)\n"
        "v_mfma_f32_16x16x32_bf16 v[40:43], v[176:179], v[24:27], v[40:43]\n"
        "ds_read_b128 v[20:23], v3 offset:12288\n"
        "v_mfma_f32_16x16x32_bf16 v[44:47], v[180:183], v[24:27], v[44:47]\n"
        "v_cvt_pk_bf16_f32 v9, v34, v35\n"
        "s_waitcnt lgkmcnt(4)\n"
        "v_mfma_f32_16x16x32_bf16 v[40:43], v[184:187], v[28:31], v[40:43]\n"
        "ds_read_b128 v[24:27], v4 offset:12288\n"
        "v_mfma_f32_16x16x32_bf16 v[44:47], v[188:191], v[28:31], v[44:47]\n"
        "v_cvt_pk_bf16_f32 v10, v36, v37\n"
        "s_waitcnt lgkmcnt(4)\n"
        "v_mfma_f32_16x16x32_bf16 v[40:43], v[192:195], v[12:15], v[40:43]\n"
        "ds_read_b128 v[28:31], v3 offset:14336\n"
        "v_mfma_f32_16x16x32_bf16 v[44:47], v[196:199], v[12:15], v[44:47]\n"
        "v_cvt_pk_bf16_f32 v11, v38, v39\n"
        "s_waitcnt lgkmcnt(3)\n"
        "v_mfma_f32_16x16x32_bf16 v[40:43], v[200:203], v[16:19], v[40:43]\n"
        "ds_read_b128 v[12:15], v4 offset:14336\n"
        "v_mfma_f32_16x16x32_bf16 v[44:47], v[204:207], v[16:19], v[44:47]\n"
        "ds_write_b128 v6, v[8:11] offset:12288\n"
        "s_waitcnt lgkmcnt(4)\n"
        "v_mfma_f32_16x16x32_bf16 v[40:43], v[208:211], v[20:23], v[40:43]\n"
        "ds_read_b128 v[16:19], v3 offset:16384\n"
        "v_mfma_f32_16x16x32_bf16 v[44:47], v[212:215], v[20:23], v[44:47]\n"
        "s_waitcnt lgkmcnt(4)\n"
        "v_mfma_f32_16x16x32_bf16 v[40:43], v[216:219], v[24:27], v[40:43]\n"
        "ds_read_b128 v[20:23], v4 offset:16384\n"
        "v_mfma_f32_16x16x32_bf16 v[44:47], v[220:223], v[24:27], v[44:47]\n"
        "s_waitcnt lgkmcnt(2)\n"
        "s_barrier\n"
        "v_subrev_u32 v3, 140544, v3\n"
        "v_subrev_u32 v4, 140544, v4\n"
        "s_waitcnt lgkmcnt(4)\n"
        "v_mfma_f32_16x16x32_bf16 v[40:43], v[224:227], v[28:31], v[40:43]\n"
        "ds_read_b128 v[24:27], v3 offset:0\n"
        "v_mfma_f32_16x16x32_bf16 v[44:47], v[228:231], v[28:31], v[44:47]\n"
        "s_waitcnt lgkmcnt(4)\n"
        "v_mfma_f32_16x16x32_bf16 v[40:43], v[232:235], v[12:15], v[40:43]\n"
        "ds_read_b128 v[28:31], v4 offset:0\n"
        "v_mfma_f32_16x16x32_bf16 v[44:47], v[236:239], v[12:15], v[44:47]\n"
        "s_waitcnt lgkmcnt(3)\n"
        "v_mfma_f32_16x16x32_bf16 v[40:43], v[240:243], v[16:19], v[40:43]\n"
        "ds_read_b128 v[12:15], v3 offset:2048\n"
        "v_mfma_f32_16x16x32_bf16 v[44:47], v[244:247], v[16:19], v[44:47]\n"
        "s_waitcnt lgkmcnt(3)\n"
        "v_mfma_f32_16x16x32_bf16 v[40:43], v[248:251], v[20:23], v[40:43]\n"
        "ds_read_b128 v[16:19], v4 offset:2048\n"
        "v_mfma_f32_16x16x32_bf16 v[44:47], v[252:255], v[20:23], v[44:47]\n"
        "s_waitcnt lgkmcnt(3)\n"
        "v_mfma_f32_16x16x32_bf16 v[32:35], v[112:115], v[24:27], 0\n"
        "ds_read_b128 v[20:23], v3 offset:4096\n"
        "v_mfma_f32_16x16x32_bf16 v[36:39], v[116:119], v[24:27], 0\n"
        "s_waitcnt lgkmcnt(3)\n"
        "v_mfma_f32_16x16x32_bf16 v[32:35], v[120:123], v[28:31], v[32:35]\n"
        "ds_read_b128 v[24:27], v4 offset:4096\n"
        "v_mfma_f32_16x16x32_bf16 v[36:39], v[124:127], v[28:31], v[36:39]\n"
        "s_waitcnt lgkmcnt(3)\n"
        "v_mfma_f32_16x16x32_bf16 v[32:35], v[128:131], v[12:15], v[32:35]\n"
        "ds_read_b128 v[28:31], v3 offset:6144\n"
        "v_mfma_f32_16x16x32_bf16 v[36:39], v[132:135], v[12:15], v[36:39]\n"
        "v_cvt_pk_bf16_f32 v8, v40, v41\n"
        "s_waitcnt lgkmcnt(3)\n"
        "v_mfma_f32_16x16x32_bf16 v[32:35], v[136:139], v[16:19], v[32:35]\n"
        "ds_read_b128 v[12:15], v4 offset:6144\n"
        "v_mfma_f32_16x16x32_bf16 v[36:39], v[140:143], v[16:19], v[36:39]\n"
        "v_cvt_pk_bf16_f32 v9, v42, v43\n"
        "s_waitcnt lgkmcnt(3)\n"
        "v_mfma_f32_16x16x32_bf16 v[32:35], v[144:147], v[20:23], v[32:35]\n"
        "ds_read_b128 v[16:19], v3 offset:8192\n"
        "v_mfma_f32_16x16x32_bf16 v[36:39], v[148:151], v[20:23], v[36:39]\n"
        "v_cvt_pk_bf16_f32 v10, v44, v45\n"
        "s_waitcnt lgkmcnt(3)\n"
        "v_mfma_f32_16x16x32_bf16 v[32:35], v[152:155], v[24:27], v[32:35]\n"
        "ds_read_b128 v[20:23], v4 offset:8192\n"
        "v_mfma_f32_16x16x32_bf16 v[36:39], v[156:159], v[24:27], v[36:39]\n"
        "v_cvt_pk_bf16_f32 v11, v46, v47\n"
        "s_waitcnt lgkmcnt(3)\n"
        "v_mfma_f32_16x16x32_bf16 v[32:35], v[160:163], v[28:31], v[32:35]\n"
        "ds_read_b128 v[24:27], v3 offset:10240\n"
        "v_mfma_f32_16x16x32_bf16 v[36:39], v[164:167], v[28:31], v[36:39]\n"
        "ds_write_b128 v6, v[8:11] offset:16384\n"
        "s_waitcnt lgkmcnt(4)\n"
        "v_mfma_f32_16x16x32_bf16 v[32:35], v[168:171], v[12:15], v[32:35]\n"
        "ds_read_b128 v[28:31], v4 offset:10240\n"
        "v_mfma_f32_16x16x32_bf16 v[36:39], v[172:175], v[12:15], v[36:39]\n"
        "s_waitcnt lgkmcnt(4)\n"
        "v_mfma_f32_16x16x32_bf16 v[32:35], v[176:179], v[16:19], v[32:35]\n"
        "ds_read_b128 v[12:15], v3 offset:12288\n"
        "v_mfma_f32_16x16x32_bf16 v[36:39], v[180:183], v[16:19], v[36:39]\n"
        "s_waitcnt lgkmcnt(4)\n"
        "v_mfma_f32_16x16x32_bf16 v[32:35], v[184:187], v[20:23], v[32:35]\n"
        "ds_read_b128 v[16:19], v4 offset:12288\n"
        "v_mfma_f32_16x16x32_bf16 v[36:39], v[188:191], v[20:23], v[36:39]\n"
        "s_waitcnt lgkmcnt(4)\n"
        "v_mfma_f32_16x16x32_bf16 v[32:35], v[192:195], v[24:27], v[32:35]\n"
        "ds_read_b128 v[20:23], v3 offset:14336\n"
        "v_mfma_f32_16x16x32_bf16 v[36:39], v[196:199], v[24:27], v[36:39]\n"
        "s_waitcnt lgkmcnt(3)\n"
        "v_mfma_f32_16x16x32_bf16 v[32:35], v[200:203], v[28:31], v[32:35]\n"
        "ds_read_b128 v[24:27], v4 offset:14336\n"
        "v_mfma_f32_16x16x32_bf16 v[36:39], v[204:207], v[28:31], v[36:39]\n"
        "s_waitcnt lgkmcnt(3)\n"
        "v_mfma_f32_16x16x32_bf16 v[32:35], v[208:211], v[12:15], v[32:35]\n"
        "ds_read_b128 v[28:31], v3 offset:16384\n"
        "v_mfma_f32_16x16x32_bf16 v[36:39], v[212:215], v[12:15], v[36:39]\n"
        "s_waitcnt lgkmcnt(3)\n"
        "v_mfma_f32_16x16x32_bf16 v[32:35], v[216:219], v[16:19], v[32:35]\n"
        "ds_read_b128 v[12:15], v4 offset:16384\n"
        "v_mfma_f32_16x16x32_bf16 v[36:39], v[220:223], v[16:19], v[36:39]\n"
        "s_waitcnt lgkmcnt(7)\n"
        "s_barrier\n"
        "v_add_u32 v3, 18432, v3\n"
        "v_add_u32 v4, 18432, v4\n"
        "s_waitcnt lgkmcnt(3)\n"
        "v_mfma_f32_16x16x32_bf16 v[32:35], v[224:227], v[20:23], v[32:35]\n"
        "ds_read_b128 v[16:19], v3 offset:0\n"
        "v_mfma_f32_16x16x32_bf16 v[36:39], v[228:231], v[20:23], v[36:39]\n"
        "s_waitcnt lgkmcnt(3)\n"
        "v_mfma_f32_16x16x32_bf16 v[32:35], v[232:235], v[24:27], v[32:35]\n"
        "ds_read_b128 v[20:23], v4 offset:0\n"
        "v_mfma_f32_16x16x32_bf16 v[36:39], v[236:239], v[24:27], v[36:39]\n"
        "s_waitcnt lgkmcnt(3)\n"
        "v_mfma_f32_16x16x32_bf16 v[32:35], v[240:243], v[28:31], v[32:35]\n"
        "ds_read_b128 v[24:27], v3 offset:2048\n"
        "v_mfma_f32_16x16x32_bf16 v[36:39], v[244:247], v[28:31], v[36:39]\n"
        "s_waitcnt lgkmcnt(3)\n"
        "v_mfma_f32_16x16x32_bf16 v[32:35], v[248:251], v[12:15], v[32:35]\n"
        "ds_read_b128 v[28:31], v4 offset:2048\n"
        "v_mfma_f32_16x16x32_bf16 v[36:39], v[252:255], v[12:15], v[36:39]\n"
        "s_waitcnt lgkmcnt(3)\n"
        "v_mfma_f32_16x16x32_bf16 v[40:43], v[112:115], v[16:19], 0\n"
        "ds_read_b128 v[12:15], v3 offset:4096\n"
        "v_mfma_f32_16x16x32_bf16 v[44:47], v[116:119], v[16:19], 0\n"
        "s_waitcnt lgkmcnt(3)\n"
        "v_mfma_f32_16x16x32_bf16 v[40:43], v[120:123], v[20:23], v[40:43]\n"
        "ds_read_b128 v[16:19], v4 offset:4096\n"
        "v_mfma_f32_16x16x32_bf16 v[44:47], v[124:127], v[20:23], v[44:47]\n"
        "s_waitcnt lgkmcnt(3)\n"
        "v_mfma_f32_16x16x32_bf16 v[40:43], v[128:131], v[24:27], v[40:43]\n"
        "ds_read_b128 v[20:23], v3 offset:6144\n"
        "v_mfma_f32_16x16x32_bf16 v[44:47], v[132:135], v[24:27], v[44:47]\n"
        "v_cvt_pk_bf16_f32 v8, v32, v33\n"
        "s_waitcnt lgkmcnt(3)\n"
        "v_mfma_f32_16x16x32_bf16 v[40:43], v[136:139], v[28:31], v[40:43]\n"
        "ds_read_b128 v[24:27], v4 offset:6144\n"
        "v_mfma_f32_16x16x32_bf16 v[44:47], v[140:143], v[28:31], v[44:47]\n"
        "v_cvt_pk_bf16_f32 v9, v34, v35\n"
        "s_waitcnt lgkmcnt(3)\n"
        "v_mfma_f32_16x16x32_bf16 v[40:43], v[144:147], v[12:15], v[40:43]\n"
        "ds_read_b128 v[28:31], v3 offset:8192\n"
        "v_mfma_f32_16x16x32_bf16 v[44:47], v[148:151], v[12:15], v[44:47]\n"
        "v_cvt_pk_bf16_f32 v10, v36, v37\n"
        "s_waitcnt lgkmcnt(3)\n"
        "v_mfma_f32_16x16x32_bf16 v[40:43], v[152:155], v[16:19], v[40:43]\n"
        "ds_read_b128 v[12:15], v4 offset:8192\n"
        "v_mfma_f32_16x16x32_bf16 v[44:47], v[156:159], v[16:19], v[44:47]\n"
        "v_cvt_pk_bf16_f32 v11, v38, v39\n"
        "s_waitcnt lgkmcnt(3)\n"
        "v_mfma_f32_16x16x32_bf16 v[40:43], v[160:163], v[20:23], v[40:43]\n"
        "ds_read_b128 v[16:19], v3 offset:10240\n"
        "v_mfma_f32_16x16x32_bf16 v[44:47], v[164:167], v[20:23], v[44:47]\n"
        "ds_write_b128 v6, v[8:11] offset:24576\n"
        "s_waitcnt lgkmcnt(4)\n"
        "v_mfma_f32_16x16x32_bf16 v[40:43], v[168:171], v[24:27], v[40:43]\n"
        "ds_read_b128 v[20:23], v4 offset:10240\n"
        "v_mfma_f32_16x16x32_bf16 v[44:47], v[172:175], v[24:27], v[44:47]\n"
        "s_waitcnt lgkmcnt(4)\n"
        "v_mfma_f32_16x16x32_bf16 v[40:43], v[176:179], v[28:31], v[40:43]\n"
        "ds_read_b128 v[24:27], v3 offset:12288\n"
        "v_mfma_f32_16x16x32_bf16 v[44:47], v[180:183], v[28:31], v[44:47]\n"
        "s_waitcnt lgkmcnt(4)\n"
        "v_mfma_f32_16x16x32_bf16 v[40:43], v[184:187], v[12:15], v[40:43]\n"
        "ds_read_b128 v[28:31], v4 offset:12288\n"
        "v_mfma_f32_16x16x32_bf16 v[44:47], v[188:191], v[12:15], v[44:47]\n"
        "s_waitcnt lgkmcnt(4)\n"
        "v_mfma_f32_16x16x32_bf16 v[40:43], v[192:195], v[16:19], v[40:43]\n"
        "ds_read_b128 v[12:15], v3 offset:14336\n"
        "v_mfma_f32_16x16x32_bf16 v[44:47], v[196:199], v[16:19], v[44:47]\n"
        "s_waitcnt lgkmcnt(3)\n"
        "v_mfma_f32_16x16x32_bf16 v[40:43], v[200:203], v[20:23], v[40:43]\n"
        "ds_read_b128 v[16:19], v4 offset:14336\n"
        "v_mfma_f32_16x16x32_bf16 v[44:47], v[204:207], v[20:23], v[44:47]\n"
        "s_waitcnt lgkmcnt(3)\n"
        "v_mfma_f32_16x16x32_bf16 v[40:43], v[208:211], v[24:27], v[40:43]\n"
        "ds_read_b128 v[20:23], v3 offset:16384\n"
        "v_mfma_f32_16x16x32_bf16 v[44:47], v[212:215], v[24:27], v[44:47]\n"
        "s_waitcnt lgkmcnt(3)\n"
        "v_mfma_f32_16x16x32_bf16 v[40:43], v[216:219], v[28:31], v[40:43]\n"
        "ds_read_b128 v[24:27], v4 offset:16384\n"
        "v_mfma_f32_16x16x32_bf16 v[44:47], v[220:223], v[28:31], v[44:47]\n"
        "s_waitcnt lgkmcnt(7)\n"
        "s_barrier\n"
        "v_add_u32 v3, 85248, v3\n"
        "v_add_u32 v4, 85248, v4\n"
        "s_waitcnt lgkmcnt(3)\n"
        "v_mfma_f32_16x16x32_bf16 v[40:43], v[224:227], v[12:15], v[40:43]\n"
        "ds_read_b128 v[28:31], v3 offset:0\n"
        "v_mfma_f32_16x16x32_bf16 v[44:47], v[228:231], v[12:15], v[44:47]\n"
        "s_waitcnt lgkmcnt(3)\n"
        "v_mfma_f32_16x16x32_bf16 v[40:43], v[232:235], v[16:19], v[40:43]\n"
        "ds_read_b128 v[12:15], v4 offset:0\n"
        "v_mfma_f32_16x16x32_bf16 v[44:47], v[236:239], v[16:19], v[44:47]\n"
        "s_waitcnt lgkmcnt(3)\n"
        "v_mfma_f32_16x16x32_bf16 v[40:43], v[240:243], v[20:23], v[40:43]\n"
        "ds_read_b128 v[16:19], v3 offset:2048\n"
        "v_mfma_f32_16x16x32_bf16 v[44:47], v[244:247], v[20:23], v[44:47]\n"
        "s_waitcnt lgkmcnt(3)\n"
        "v_mfma_f32_16x16x32_bf16 v[40:43], v[248:251], v[24:27], v[40:43]\n"
        "ds_read_b128 v[20:23], v4 offset:2048\n"
        "v_mfma_f32_16x16x32_bf16 v[44:47], v[252:255], v[24:27], v[44:47]\n"
        "s_waitcnt lgkmcnt(3)\n"
        "v_mfma_f32_16x16x32_bf16 v[32:35], v[112:115], v[28:31], 0\n"
        "ds_read_b128 v[24:27], v3 offset:4096\n"
        "v_mfma_f32_16x16x32_bf16 v[36:39], v[116:119], v[28:31], 0\n"
        "s_waitcnt lgkmcnt(3)\n"
        "v_mfma_f32_16x16x32_bf16 v[32:35], v[120:123], v[12:15], v[32:35]\n"
        "ds_read_b128 v[28:31], v4 offset:4096\n"
        "v_mfma_f32_16x16x32_bf16 v[36:39], v[124:127], v[12:15], v[36:39]\n"
        "s_waitcnt lgkmcnt(3)\n"
        "v_mfma_f32_16x16x32_bf16 v[32:35], v[128:131], v[16:19], v[32:35]\n"
        "ds_read_b128 v[12:15], v3 offset:6144\n"
        "v_mfma_f32_16x16x32_bf16 v[36:39], v[132:135], v[16:19], v[36:39]\n"
        "v_cvt_pk_bf16_f32 v8, v40, v41\n"
        "s_waitcnt lgkmcnt(3)\n"
        "v_mfma_f32_16x16x32_bf16 v[32:35], v[136:139], v[20:23], v[32:35]\n"
        "ds_read_b128 v[16:19], v4 offset:6144\n"
        "v_mfma_f32_16x16x32_bf16 v[36:39], v[140:143], v[20:23], v[36:39]\n"
        "v_cvt_pk_bf16_f32 v9, v42, v43\n"
        "s_waitcnt lgkmcnt(3)\n"
        "v_mfma_f32_16x16x32_bf16 v[32:35], v[144:147], v[24:27], v[32:35]\n"
        "ds_read_b128 v[20:23], v3 offset:8192\n"
        "v_mfma_f32_16x16x32_bf16 v[36:39], v[148:151], v[24:27], v[36:39]\n"
        "v_cvt_pk_bf16_f32 v10, v44, v45\n"
        "s_waitcnt lgkmcnt(3)\n"
        "v_mfma_f32_16x16x32_bf16 v[32:35], v[152:155], v[28:31], v[32:35]\n"
        "ds_read_b128 v[24:27], v4 offset:8192\n"
        "v_mfma_f32_16x16x32_bf16 v[36:39], v[156:159], v[28:31], v[36:39]\n"
        "v_cvt_pk_bf16_f32 v11, v46, v47\n"
        "s_waitcnt lgkmcnt(3)\n"
        "v_mfma_f32_16x16x32_bf16 v[32:35], v[160:163], v[12:15], v[32:35]\n"
        "ds_read_b128 v[28:31], v3 offset:10240\n"
        "v_mfma_f32_16x16x32_bf16 v[36:39], v[164:167], v[12:15], v[36:39]\n"
        "ds_write_b128 v6, v[8:11] offset:32768\n"
        "s_waitcnt lgkmcnt(4)\n"
        "v_mfma_f32_16x16x32_bf16 v[32:35], v[168:171], v[16:19], v[32:35]\n"
        "ds_read_b128 v[12:15], v4 offset:10240\n"
        "v_mfma_f32_16x16x32_bf16 v[36:39], v[172:175], v[16:19], v[36:39]\n"
        "s_waitcnt lgkmcnt(4)\n"
        "v_mfma_f32_16x16x32_bf16 v[32:35], v[176:179], v[20:23], v[32:35]\n"
        "ds_read_b128 v[16:19], v3 offset:12288\n"
        "v_mfma_f32_16x16x32_bf16 v[36:39], v[180:183], v[20:23], v[36:39]\n"
        "s_waitcnt lgkmcnt(2)\n"
        "s_barrier\n"
        "s_waitcnt lgkmcnt(4)\n"
        "v_mfma_f32_16x16x32_bf16 v[32:35], v[184:187], v[24:27], v[32:35]\n"
        "ds_read_b128 v[20:23], v4 offset:12288\n"
        "v_mfma_f32_16x16x32_bf16 v[36:39], v[188:191], v[24:27], v[36:39]\n"
        "s_waitcnt lgkmcnt(4)\n"
        "v_mfma_f32_16x16x32_bf16 v[32:35], v[192:195], v[28:31], v[32:35]\n"
        "ds_read_b128 v[24:27], v3 offset:14336\n"
        "v_mfma_f32_16x16x32_bf16 v[36:39], v[196:199], v[28:31], v[36:39]\n"
        "s_waitcnt lgkmcnt(3)\n"
        "v_mfma_f32_16x16x32_bf16 v[32:35], v[200:203], v[12:15], v[32:35]\n"
        "ds_read_b128 v[28:31], v4 offset:14336\n"
        "v_mfma_f32_16x16x32_bf16 v[36:39], v[204:207], v[12:15], v[36:39]\n"
        "s_waitcnt lgkmcnt(3)\n"
        "v_mfma_f32_16x16x32_bf16 v[32:35], v[208:211], v[16:19], v[32:35]\n"
        "ds_read_b128 v[12:15], v3 offset:16384\n"
        "v_mfma_f32_16x16x32_bf16 v[36:39], v[212:215], v[16:19], v[36:39]\n"
        "s_waitcnt lgkmcnt(3)\n"
        "v_mfma_f32_16x16x32_bf16 v[32:35], v[216:219], v[20:23], v[32:35]\n"
        "ds_read_b128 v[16:19], v4 offset:16384\n"
        "v_mfma_f32_16x16x32_bf16 v[36:39], v[220:223], v[20:23], v[36:39]\n"
        "s_waitcnt lgkmcnt(3)\n"
        "v_mfma_f32_16x16x32_bf16 v[32:35], v[224:227], v[24:27], v[32:35]\n"
        "v_mfma_f32_16x16x32_bf16 v[36:39], v[228:231], v[24:27], v[36:39]\n"
        "s_waitcnt lgkmcnt(2)\n"
        "v_mfma_f32_16x16x32_bf16 v[32:35], v[232:235], v[28:31], v[32:35]\n"
        "v_mfma_f32_16x16x32_bf16 v[36:39], v[236:239], v[28:31], v[36:39]\n"
        "s_waitcnt lgkmcnt(1)\n"
        "v_mfma_f32_16x16x32_bf16 v[32:35], v[240:243], v[12:15], v[32:35]\n"
        "v_mfma_f32_16x16x32_bf16 v[36:39], v[244:247], v[12:15], v[36:39]\n"
        "s_waitcnt lgkmcnt(0)\n"
        "v_mfma_f32_16x16x32_bf16 v[32:35], v[248:251], v[16:19], v[32:35]\n"
        "v_mfma_f32_16x16x32_bf16 v[36:39], v[252:255], v[16:19], v[36:39]\n"
        "s_nop 7\n"
        "v_cvt_pk_bf16_f32 v8, v32, v33\n"
        "v_cvt_pk_bf16_f32 v9, v34, v35\n"
        "v_cvt_pk_bf16_f32 v10, v36, v37\n"
        "v_cvt_pk_bf16_f32 v11, v38, v39\n"
        "buffer_store_dwordx4 v[8:11], v7, s[52:55], s85 offen sc1 nt\n"
        "L_end_%=:\n"
        :
        : "s"(ka), "s"(blk), "s"(wid), "s"(ldsb)
        : "v0", "v1", "v2", "v3", "v4", "v5", "v6", "v7", "v8", "v9", "v10", "v11", "v12", "v13", "v14", "v15", "v16", "v17", "v18", "v19", "v20", "v21", "v22", "v23", "v24", "v25", "v26", "v27", "v28", "v29", "v30", "v31", "v32", "v33", "v34", "v35", "v36", "v37", "v38", "v39", "v40", "v41", "v42", "v43", "v44", "v45", "v46", "v47", "v48", "v49", "v50", "v51", "v52", "v53", "v54", "v55", "v56", "v57", "v58", "v59", "v60", "v61", "v62", "v63", "v64", "v65", "v66", "v67", "v68", "v69", "v70", "v71", "v72", "v73", "v74", "v75", "v76", "v77", "v78", "v79", "v80", "v81", "v82", "v83", "v84", "v85", "v86", "v87", "v88", "v89", "v90", "v91", "v92", "v93", "v94", "v95", "v96", "v97", "v98", "v99", "v100", "v101", "v102", "v103", "v104", "v105", "v106", "v107", "v108", "v109", "v110", "v111", "v112", "v113", "v114", "v115", "v116", "v117", "v118", "v119", "v120", "v121", "v122", "v123", "v124", "v125", "v126", "v127", "v128", "v129", "v130", "v131", "v132", "v133", "v134", "v135", "v136", "v137", "v138", "v139", "v140", "v141", "v142", "v143", "v144", "v145", "v146", "v147", "v148", "v149", "v150", "v151", "v152", "v153", "v154", "v155", "v156", "v157", "v158", "v159", "v160", "v161", "v162", "v163", "v164", "v165", "v166", "v167", "v168", "v169", "v170", "v171", "v172", "v173", "v174", "v175", "v176", "v177", "v178", "v179", "v180", "v181", "v182", "v183", "v184", "v185", "v186", "v187", "v188", "v189", "v190", "v191", "v192", "v193", "v194", "v195", "v196", "v197", "v198", "v199", "v200", "v201", "v202", "v203", "v204", "v205", "v206", "v207", "v208", "v209", "v210", "v211", "v212", "v213", "v214", "v215", "v216", "v217", "v218", "v219", "v220", "v221", "v222", "v223", "v224", "v225", "v226", "v227", "v228", "v229", "v230", "v231", "v232", "v233", "v234", "v235", "v236", "v237", "v238", "v239", "v240", "v241", "v242", "v243", "v244", "v245", "v246", "v247", "v248", "v249", "v250", "v251", "v252", "v253", "v254", "v255", "s12", "s13", "s14", "s15", "s16", "s17", "s18", "s19", "s20", "s21", "s22", "s23", "s24", "s25", "s26", "s27", "s28", "s29", "s30", "s31", "s33", "s34", "s35", "s36", "s37", "s38", "s39", "s40", "s41", "s42", "s43", "s44", "s45", "s46", "s47", "s48", "s49", "s50", "s51", "s52", "s53", "s54", "s55", "s56", "s57", "s58", "s59", "s60", "s61", "s62", "s63", "s64", "s65", "s66", "s67", "s68", "s69", "s70", "s71", "s72", "s73", "s74", "s75", "s76", "s77", "s78", "s79", "s80", "s81", "s82", "s83", "s84", "s85", "s86", "s87", "s88", "s89", "s90", "s91", "s92", "s93", "s94", "s95", "s96", "s97", "s98", "s99", "vcc", "scc", "memory");
}

extern "C" void kernel_launch(void* const* d_in, const int* in_sizes, int n_in, void* d_out, int out_size,
                              void* d_ws, size_t ws_size, hipStream_t stream) {
    oq_kernel<<<256, 512, 0, stream>>>(d_in[0], d_in[1], d_in[2], d_in[3], d_in[4], d_out);
}
```
